```python
import torch

def _softmax(x, axis=-1):
    return torch.softmax(x, dim=axis)
def _gelu(x):
    return torch.nn.functional.gelu(x, approximate="tanh")
def _silu(x):
    return torch.nn.functional.silu(x)
def _sigmoid(x):
    return torch.sigmoid(x)
def _layernorm(x, axis=-1, eps=1e-5):
    m = x.mean(dim=axis, keepdim=True)
    v = x.var(dim=axis, keepdim=True, unbiased=False)
    return (x - m) / torch.sqrt(v + eps)
def _scatter_add(tgt, idx, src, axis=0):
    return tgt.clone().index_add_(axis, idx, src)
def _segment_sum(src, seg, n):
    out = torch.zeros((n,) + src.shape[1:], dtype=src.dtype)
    return out.index_add_(0, seg, src)
def _pad(x, pw):
    flat = tuple(v for pair in reversed(pw) for v in pair)
    return torch.nn.functional.pad(x, flat)

kernel_name = 'sequential_preLN__compressed_kv_cla__swiglu_ffn__decode'

def setup_inputs():

    _w = (lambda a: (lambda t: t.to(torch.bfloat16) if t.is_floating_point() else t)(torch.as_tensor(a)))
    def _A_setup():
        B, H, Sq, Sk, D, M = 2, 32, 1, 4096, 128, 576
        Dc = 4 * D
        return {
            "x": _w((lambda *s: torch.randn(*s, dtype=torch.bfloat16) * 0.05)(B, Sq, M)),
            "c_kv": _w((lambda *s: torch.randn(*s, dtype=torch.bfloat16) * 0.4)(B, Sk, Dc)),
            "w_q": _w((lambda *s: torch.randn(*s, dtype=torch.bfloat16) * 3.2)(M, H, D) * (M ** -0.5)),
            "w_uk": _w((lambda *s: torch.randn(*s, dtype=torch.bfloat16) * 3.2)(Dc, H, D) * (Dc ** -0.5)),
            "w_uv": _w((lambda *s: torch.randn(*s, dtype=torch.bfloat16) * 0.4)(Dc, H, D) * (Dc ** -0.5)),
            "w_o": _w((lambda *s: torch.randn(*s, dtype=torch.bfloat16) * 0.4)(H, D, M) * ((H * D) ** -0.5)),
        }
    _A_in = _A_setup()

    def _B_setup():
        B, S, D, F = 2, 1, 576, 4608
        return {
            "x": _w((lambda *s: torch.randn(*s, dtype=torch.bfloat16) * 0.05)(B, S, D)),
            "w_gate": _w((lambda *s: torch.randn(*s, dtype=torch.bfloat16) * 0.05)(D, F)),
            "w_up": _w((lambda *s: torch.randn(*s, dtype=torch.bfloat16) * 0.05)(D, F)),
            "w_down": _w((lambda *s: torch.randn(*s, dtype=torch.bfloat16) * 0.05)(F, D)),
        }
    _B_in = _B_setup()

    return {
        "A_x": _A_in["x"],
        "A_c_kv": _A_in["c_kv"],
        "A_w_q": _A_in["w_q"],
        "A_w_uk": _A_in["w_uk"],
        "A_w_uv": _A_in["w_uv"],
        "A_w_o": _A_in["w_o"],
        "B_x": _B_in["x"],
        "B_w_gate": _B_in["w_gate"],
        "B_w_up": _B_in["w_up"],
        "B_w_down": _B_in["w_down"],
    }

def reference(A_x, A_c_kv, A_w_q, A_w_uk, A_w_uv, A_w_o, B_x, B_w_gate, B_w_up, B_w_down):
    def _A_ref(x, c_kv, w_q, w_uk, w_uv, w_o):
        B, Sq, M = x.shape[0], x.shape[1], x.shape[2]
        Sk = c_kv.shape[1]
        H, D = w_q.shape[1], w_q.shape[2]
        q = torch.einsum("bqm,mhd->bhqd", x, w_q)
        k = torch.einsum("bkc,chd->bhkd", c_kv, w_uk)
        v = torch.einsum("bkc,chd->bhkd", c_kv, w_uv)
        scores = torch.einsum("bhqd,bhkd->bhqk", q, k) * (D ** -0.5)
        off = Sk - Sq
        qi = torch.arange(Sq)[:, None]
        kj = torch.arange(Sk)[None, :]
        cmask = kj <= (qi + off)
        scores = torch.where(cmask[None, None], scores, -1e9)
        p = _softmax(scores, axis=-1)
        ctx = torch.einsum("bhqk,bhkd->bhqd", p, v)
        return torch.einsum("bhqd,hdm->bqm", ctx, w_o)

    def _B_ref(x, w_gate, w_up, w_down):
        g = _silu(x @ w_gate)
        u = x @ w_up
        return (g * u) @ w_down

    x = A_x
    h = x + _A_ref(_layernorm(x, axis=-1), A_c_kv, A_w_q, A_w_uk, A_w_uv, A_w_o)
    h2 = h + _B_ref(_layernorm(h, axis=-1), B_w_gate, B_w_up, B_w_down)
    return h2
```

```cpp
#include <hip/hip_runtime.h>
#include <hip/hip_bf16.h>
#include <stdint.h>

typedef unsigned short bf16_t;
__device__ __forceinline__ float bf2f(bf16_t b) { return __builtin_bit_cast(float, (unsigned)b << 16); }
__device__ __forceinline__ bf16_t f2bf(float f) { __hip_bfloat16 h = __float2bfloat16(f); return __builtin_bit_cast(bf16_t, h); }

constexpr int B = 2, H = 32, SK = 4096, D = 128, M = 576, DC = 512, F = 4608;
constexpr int NS = 64;

__device__ float block_sum(float v, float* red) {
  for (int o = 32; o > 0; o >>= 1) v += __shfl_xor(v, o);
  __syncthreads();
  if ((threadIdx.x & 63) == 0) red[threadIdx.x >> 6] = v;
  __syncthreads();
  float s = 0;
  for (int i = 0; i < (int)(blockDim.x >> 6); ++i) s += red[i];
  return s;
}

__global__ void __launch_bounds__(256) k1(const bf16_t* x, const bf16_t* wq, const bf16_t* wuk, float* qabs) {
  __shared__ float xn[B][M];
  __shared__ float q[B][D];
  __shared__ float red[16];
  int h = blockIdx.x, t = threadIdx.x;
  for (int b = 0; b < B; ++b) {
    float s = 0;
    for (int i = t; i < M; i += 256) s += bf2f(x[b * M + i]);
    float mean = block_sum(s, red) / M;
    float v = 0;
    for (int i = t; i < M; i += 256) { float d = bf2f(x[b * M + i]) - mean; v += d * d; }
    float var = block_sum(v, red) / M;
    float r = 1.0f / sqrtf(var + 1e-5f);
    for (int i = t; i < M; i += 256) xn[b][i] = (bf2f(x[b * M + i]) - mean) * r;
  }
  __syncthreads();
  {
    int b = t >> 7, d = t & 127;
    float acc = 0;
    for (int m = 0; m < M; ++m) acc += xn[b][m] * bf2f(wq[(size_t)m * H * D + h * D + d]);
    q[b][d] = acc;
  }
  __syncthreads();
  for (int i = t; i < B * DC; i += 256) {
    int b = i / DC, c = i % DC;
    float acc = 0;
    for (int d = 0; d < D; ++d) acc += q[b][d] * bf2f(wuk[(size_t)c * H * D + h * D + d]);
    qabs[(b * H + h) * DC + c] = acc * 0.08838834764831845f;
  }
}

__global__ void __launch_bounds__(256) k2(const bf16_t* ckv, const float* qabs, float* part, float* stats) {
  constexpr int KS = SK / NS;
  __shared__ float sc[H][KS];
  __shared__ float mx[H];
  int s = blockIdx.x, b = blockIdx.y, t = threadIdx.x;
  const bf16_t* kv = ckv + ((size_t)b * SK + s * KS) * DC;
  for (int i = t; i < H * KS; i += 256) {
    int h = i / KS, k = i % KS;
    float acc = 0;
    for (int c = 0; c < DC; ++c) acc += qabs[(b * H + h) * DC + c] * bf2f(kv[k * DC + c]);
    sc[h][k] = acc;
  }
  __syncthreads();
  if (t < H) {
    float m = -INFINITY;
    for (int k = 0; k < KS; ++k) m = fmaxf(m, sc[t][k]);
    float l = 0;
    for (int k = 0; k < KS; ++k) { float p = __expf(sc[t][k] - m); sc[t][k] = p; l += p; }
    stats[((b * H + t) * NS + s) * 2] = m;
    stats[((b * H + t) * NS + s) * 2 + 1] = l;
  }
  __syncthreads();
  for (int i = t; i < H * DC; i += 256) {
    int h = i / DC, c = i % DC;
    float acc = 0;
    for (int k = 0; k < KS; ++k) acc += sc[h][k] * bf2f(kv[k * DC + c]);
    part[((size_t)(b * H + h) * NS + s) * DC + c] = acc;
  }
}

__global__ void __launch_bounds__(256) k3(const float* part, const float* stats, const bf16_t* wuv, const bf16_t* wo, float* opart) {
  __shared__ float cc[B][DC];
  __shared__ float ctx[B][D];
  __shared__ float wgt[B][NS];
  int h = blockIdx.x, t = threadIdx.x;
  if (t < B) {
    int b = t;
    float m = -INFINITY;
    for (int s = 0; s < NS; ++s) m = fmaxf(m, stats[((b * H + h) * NS + s) * 2]);
    float l = 0;
    for (int s = 0; s < NS; ++s) { float w = __expf(stats[((b * H + h) * NS + s) * 2] - m); wgt[b][s] = w; l += w * stats[((b * H + h) * NS + s) * 2 + 1]; }
    for (int s = 0; s < NS; ++s) wgt[b][s] /= l;
  }
  __syncthreads();
  for (int i = t; i < B * DC; i += 256) {
    int b = i / DC, c = i % DC;
    float acc = 0;
    for (int s = 0; s < NS; ++s) acc += wgt[b][s] * part[((size_t)(b * H + h) * NS + s) * DC + c];
    cc[b][c] = acc;
  }
  __syncthreads();
  {
    int b = t >> 7, d = t & 127;
    float acc = 0;
    for (int c = 0; c < DC; ++c) acc += cc[b][c] * bf2f(wuv[(size_t)c * H * D + h * D + d]);
    ctx[b][d] = acc;
  }
  __syncthreads();
  for (int i = t; i < B * M; i += 256) {
    int b = i / M, m = i % M;
    float acc = 0;
    for (int d = 0; d < D; ++d) acc += ctx[b][d] * bf2f(wo[(size_t)(h * D + d) * M + m]);
    opart[(h * B + b) * M + m] = acc;
  }
}

__global__ void __launch_bounds__(256) k4(const bf16_t* x, const float* opart, const bf16_t* wg, const bf16_t* wu, const bf16_t* wd, float* hbuf, float* fpart) {
  __shared__ float hn[B][M];
  __shared__ float act[B][18];
  __shared__ float red[16];
  int j = blockIdx.x, t = threadIdx.x;
  for (int i = t; i < B * M; i += 256) {
    int b = i / M, m = i % M;
    float acc = bf2f(x[i]);
    for (int h = 0; h < H; ++h) acc += opart[(h * B + b) * M + m];
    hn[b][m] = acc;
    if (j == 0) hbuf[i] = acc;
  }
  __syncthreads();
  for (int b = 0; b < B; ++b) {
    float s = 0;
    for (int i = t; i < M; i += 256) s += hn[b][i];
    float mean = block_sum(s, red) / M;
    float v = 0;
    for (int i = t; i < M; i += 256) { float d = hn[b][i] - mean; v += d * d; }
    float var = block_sum(v, red) / M;
    float r = 1.0f / sqrtf(var + 1e-5f);
    for (int i = t; i < M; i += 256) hn[b][i] = (hn[b][i] - mean) * r;
  }
  __syncthreads();
  if (t < B * 18) {
    int b = t / 18, f = j * 18 + t % 18;
    float g = 0, u = 0;
    for (int m = 0; m < M; ++m) { g += hn[b][m] * bf2f(wg[(size_t)m * F + f]); u += hn[b][m] * bf2f(wu[(size_t)m * F + f]); }
    act[b][t % 18] = g / (1.0f + __expf(-g)) * u;
  }
  __syncthreads();
  for (int i = t; i < B * M; i += 256) {
    int b = i / M, m = i % M;
    float acc = 0;
    for (int f = 0; f < 18; ++f) acc += act[b][f] * bf2f(wd[(size_t)(j * 18 + f) * M + m]);
    fpart[(size_t)j * B * M + i] = acc;
  }
}

__global__ void __launch_bounds__(256) k5(const float* hbuf, const float* fpart, bf16_t* out) {
  int i = blockIdx.x * 64 + threadIdx.x;
  if (i < B * M) {
    float acc = hbuf[i];
    for (int j = 0; j < 256; ++j) acc += fpart[(size_t)j * B * M + i];
    out[i] = f2bf(acc);
  }
}

extern "C" void kernel_launch(void* const* d_in, const int* in_sizes, int n_in,
                              void* d_out, int out_size, void* d_ws, size_t ws_size,
                              hipStream_t stream) {
  const bf16_t* x = (const bf16_t*)d_in[0];
  const bf16_t* ckv = (const bf16_t*)d_in[1];
  const bf16_t* wq = (const bf16_t*)d_in[2];
  const bf16_t* wuk = (const bf16_t*)d_in[3];
  const bf16_t* wuv = (const bf16_t*)d_in[4];
  const bf16_t* wo = (const bf16_t*)d_in[5];
  const bf16_t* wg = (const bf16_t*)d_in[7];
  const bf16_t* wu = (const bf16_t*)d_in[8];
  const bf16_t* wd = (const bf16_t*)d_in[9];
  float* ws = (float*)d_ws;
  float* qabs = ws;
  float* part = qabs + B * H * DC;
  float* stats = part + (size_t)B * H * NS * DC;
  float* opart = stats + B * H * NS * 2;
  float* hbuf = opart + H * B * M;
  float* fpart = hbuf + B * M;
  k1<<<H, 256, 0, stream>>>(x, wq, wuk, qabs);
  k2<<<dim3(NS, B), 256, 0, stream>>>(ckv, qabs, part, stats);
  k3<<<H, 256, 0, stream>>>(part, stats, wuv, wo, opart);
  k4<<<256, 256, 0, stream>>>(x, opart, wg, wu, wd, hbuf, fpart);
  k5<<<18, 64, 0, stream>>>(hbuf, fpart, (bf16_t*)d_out);
}
```

```cpp
#include <hip/hip_runtime.h>
#include <hip/hip_bf16.h>
#include <stdint.h>

namespace {
#define GAS __attribute__((address_space(1)))
#define LAS __attribute__((address_space(3)))
typedef unsigned short u16;
typedef unsigned v4u __attribute__((ext_vector_type(4)));
typedef unsigned v2u __attribute__((ext_vector_type(2)));
typedef float f32x4 __attribute__((ext_vector_type(4)));
typedef float f32x2 __attribute__((ext_vector_type(2)));
typedef short s16x4 __attribute__((ext_vector_type(4)));
typedef __bf16 bf16x8 __attribute__((ext_vector_type(8)));
typedef __bf16 bf16x2 __attribute__((ext_vector_type(2)));
typedef __amdgpu_buffer_rsrc_t rsrc_t;

constexpr int NTHR = 512;
constexpr int M = 576;
constexpr int QP_OFF = 4096;
constexpr int QA_OFF = QP_OFF + 32 * 8 * 2048;
constexpr int ST_OFF = QA_OFF + 2 * 32 * 2048;
constexpr int PART_OFF = ST_OFF + 64 * 32 * 16;
constexpr int OP_OFF = PART_OFF + 32 * 2 * 64 * 512;
constexpr int HB_OFF = OP_OFF + 128 * 4608;
constexpr int FP_OFF = HB_OFF + 4608;
constexpr int WS_BYTES = FP_OFF + 72 * 4608;
constexpr int STAMP_OFF = 32 << 20;
constexpr int B_QAB = 131072, B_MX = B_QAB + 16 * 1040, B_LS = B_MX + 512, B_PB = B_LS + 512, B_END = B_PB + 4096;
constexpr int F_SLAB = 0, F_G = 73728, F_U = F_G + 40960, F_G2 = 0, F_U2 = 32768;
constexpr int F_HB = F_U + 40960, F_HBS = 1184, F_ST = F_HB + 3 * F_HBS, F_ACT = F_ST + 128, F_END = F_ACT + 256;
constexpr int A_WUK = 0, A_WUV = 32768, A_WO = 65536, A_Q = A_WO + 73728, A_CC = A_Q + 512, A_CTX = A_CC + 1024, A_RED = A_CTX + 256;
constexpr int LDS_BYTES = F_END > B_END ? F_END : B_END;
static_assert(LDS_BYTES <= 163840, "LDS");
constexpr unsigned SENT = 0xFFFFFFFFu;
constexpr unsigned SPIN_CAP = 1u << 18;
constexpr int AUX_SC1 = 16, AUX_NT = 2;

struct Params {
  const u16 *x, *wq, *wuk, *ckv, *wuv, *wo, *wg, *wu, *wd;
  u16* out;
  unsigned char* ws;
};
static_assert(sizeof(Params) == 88, "no padding");

#define DEV __device__ __forceinline__
DEV float bf_lo(unsigned u) { return __builtin_bit_cast(float, u << 16); }
DEV float bf_hi(unsigned u) { return __builtin_bit_cast(float, u & 0xFFFF0000u); }
DEV unsigned fbits(float f) { return __builtin_bit_cast(unsigned, f); }
DEV float asf(unsigned u) { return __builtin_bit_cast(float, u); }
DEV unsigned pk_bf16(float a, float b) {
  f32x2 v = {a, b};
  return __builtin_bit_cast(unsigned, __builtin_convertvector(v, bf16x2));
}
DEV float sum2(unsigned pair, float acc) {
  return __builtin_amdgcn_fdot2_f32_bf16(__builtin_bit_cast(bf16x2, pair), __builtin_bit_cast(bf16x2, 0x3F803F80u), acc, false);
}
DEV f32x4 mfma(v4u a, v4u b, f32x4 c) {
  return __builtin_amdgcn_mfma_f32_16x16x32_bf16(__builtin_bit_cast(bf16x8, a), __builtin_bit_cast(bf16x8, b), c, 0, 0, 0);
}
DEV rsrc_t mkrs(const void* p, int bytes) { return __builtin_amdgcn_make_buffer_rsrc((void*)p, 0, bytes, 0x00020000); }
DEV void dma(rsrc_t rs, LAS unsigned char* dst, int voff, int soff) {
  __builtin_amdgcn_raw_ptr_buffer_load_lds(rs, (LAS void*)dst, 16, voff, soff, 0, AUX_NT);
}
DEV v4u ld_sc1(rsrc_t rs, int voff, int soff) { return __builtin_amdgcn_raw_buffer_load_b128(rs, voff, soff, AUX_SC1); }
DEV void st_sc1(rsrc_t rs, int voff, int soff, v4u v) { __builtin_amdgcn_raw_buffer_store_b128(v, rs, voff, soff, AUX_SC1); }
DEV void bar() { asm volatile("s_waitcnt lgkmcnt(0)\n\ts_barrier" ::: "memory"); }
DEV void drain() { asm volatile("s_waitcnt vmcnt(0)" ::: "memory"); }
DEV void cfence() { asm volatile("" ::: "memory"); }
template <int CTRL> DEV float dpp(float v) {
  return __builtin_bit_cast(float, __builtin_amdgcn_update_dpp(0, __builtin_bit_cast(int, v), CTRL, 0xf, 0xf, false));
}
DEV float row_sum16(float v) { v += dpp<0x128>(v); v += dpp<0x124>(v); v += dpp<0x122>(v); v += dpp<0x121>(v); return v; }
DEV float rl(float v, int l) { return __builtin_bit_cast(float, __builtin_amdgcn_readlane(__builtin_bit_cast(int, v), l)); }
DEV void x16(float v, float& a, float& b) {
  auto r = __builtin_amdgcn_permlane16_swap(fbits(v), fbits(v), false, false);
  const unsigned r0 = r[0], r1 = r[1];
  a = asf(r0); b = asf(r1);
}
DEV void x32(float v, float& a, float& b) {
  auto r = __builtin_amdgcn_permlane32_swap(fbits(v), fbits(v), false, false);
  const unsigned r0 = r[0], r1 = r[1];
  a = asf(r0); b = asf(r1);
}
DEV float sum_g(float v) { float a, b; x16(v, a, b); v = a + b; x32(v, a, b); return a + b; }
DEV float max_g(float v) { float a, b; x16(v, a, b); v = fmaxf(a, b); x32(v, a, b); return fmaxf(a, b); }
DEV v4u tr_frag(LAS unsigned char* a0, LAS unsigned char* a1) {
  const s16x4 lo = __builtin_amdgcn_ds_read_tr16_b64_v4i16((LAS s16x4*)a0);
  const s16x4 hi = __builtin_amdgcn_ds_read_tr16_b64_v4i16((LAS s16x4*)a1);
  const v2u l2 = __builtin_bit_cast(v2u, lo), h2 = __builtin_bit_cast(v2u, hi);
  return v4u{l2.x, l2.y, h2.x, h2.y};
}
DEV unsigned umin(unsigned a, unsigned b) { return a < b ? a : b; }
DEV unsigned umax(unsigned a, unsigned b) { return a > b ? a : b; }
DEV unsigned nosent(unsigned u) { return umin(u, 0xFFFFFFFEu); }
DEV unsigned vmax4(v4u v) { return umax(umax(v.x, v.y), umax(v.z, v.w)); }
DEV unsigned tagdiff(v4u v, unsigned tag) { return (v.y ^ tag) | (v.w ^ tag); }

#define STAMP(i) do {} while (0)
#define STAMP_DECL
#define STAMP_FLUSH do {} while (0)

DEV void dma_slab(rsrc_t rs, int sbase, LAS unsigned char* dst, int w, int lane, int rot) {
#pragma unroll
  for (int i = 0; i < 9; ++i) {
    int ii = i + rot;
    ii = ii >= 9 ? ii - 9 : ii;
    const int n = w + 8 * ii;
    const unsigned P = 64 * n + lane, row = (P * 58255u) >> 22;
    const unsigned s2 = (row & 2u) | ((row >> 1) & 4u);
    dma(rs, dst + n * 1024, (int)((P ^ s2) << 4), sbase);
  }
}
DEV void slab_frags(LAS unsigned char* base, int w, int lane, v4u (&a)[5][2]) {
  const int g = lane >> 4, q = (lane >> 2) & 3, pp = lane & 3;
  const int s = ((q >> 1) & 1) | ((g & 1) << 1);
  LAS unsigned char* b0 = base + (8 * g + q) * 1152 + 32 * (w ^ s) + 8 * pp;
  LAS unsigned char* b4 = base + (8 * g + q) * 1152 + 32 * (((w & 3) ^ s) + 32) + 8 * pp;
#pragma unroll
  for (int i = 0; i < 4; ++i)
#pragma unroll
    for (int ks = 0; ks < 2; ++ks) a[i][ks] = tr_frag(b0 + 256 * i + ks * 32 * 1152, b0 + 256 * i + ks * 32 * 1152 + 4 * 1152);
#pragma unroll
  for (int ks = 0; ks < 2; ++ks) a[4][ks] = tr_frag(b4 + ks * 32 * 1152, b4 + ks * 32 * 1152 + 4 * 1152);
}
DEV void slab_gemv(const v4u (&a)[5][2], LAS unsigned char* vec, rsrc_t rs, int obase, int w, int lane) {
  const int g = lane >> 4, j = lane & 15;
  const v4u b0 = *(LAS v4u*)(vec + (j & 1) * 128 + 16 * g), b1 = *(LAS v4u*)(vec + (j & 1) * 128 + 64 + 16 * g);
  f32x4 acc[5];
#pragma unroll
  for (int i = 0; i < 5; ++i) {
    acc[i] = mfma(a[i][0], b0, f32x4{0.f, 0.f, 0.f, 0.f});
    acc[i] = mfma(a[i][1], b1, acc[i]);
  }
  if (j < 2) {
    const int o = j * 2304 + 16 * g;
#pragma unroll
    for (int i = 0; i < 5; ++i) {
      if (i < 4 || w < 4) {
        v4u v = {nosent(fbits(acc[i].x)), nosent(fbits(acc[i].y)), nosent(fbits(acc[i].z)), nosent(fbits(acc[i].w))};
        st_sc1(rs, o + 512 * i, obase + 64 * w, v);
      }
    }
  }
}
template <int NP>
DEV float reduce_lines(rsrc_t rs, int base, int rr, LAS unsigned char* red, int tid, int w, int lane) {
  const int pc = tid & 7, pr = tid >> 3;
  const bool two = pr + 64 < NP;
  const int o0 = pr * 4608 + pc * 16, o1 = two ? o0 + 64 * 4608 : o0;
  v4u v0, v1;
  for (unsigned spin = 0; spin < SPIN_CAP; ++spin) {
    cfence();
    v0 = ld_sc1(rs, o0, base + rr * 128);
    v1 = ld_sc1(rs, o1, base + rr * 128);
    if (__all(umax(vmax4(v0), vmax4(v1)) != SENT)) break;
  }
  f32x4 s = __builtin_bit_cast(f32x4, v0);
  if (NP == 128 || two) s += __builtin_bit_cast(f32x4, v1);
#pragma unroll
  for (int e = 0; e < 4; ++e) {
    float t = s[e];
    t += dpp<0x128>(t);
    s[e] = sum_g(t);
  }
  if (lane < 8) *(LAS f32x4*)(red + w * 128 + lane * 16) = s;
  bar();
  float r = 0.f;
  if (w == 0) {
#pragma unroll
    for (int ww = 0; ww < 8; ++ww) r += *(LAS float*)(red + ww * 128 + (lane & 31) * 4);
  }
  return r;
}

__global__ void __launch_bounds__(NTHR, 2) fused_kernel(Params p) {
  extern __shared__ __attribute__((aligned(16))) unsigned char smem[];
  LAS unsigned char* L = (LAS unsigned char*)smem;
  const int tid = threadIdx.x, lane = tid & 63, w = __builtin_amdgcn_readfirstlane(tid >> 6), wg = blockIdx.x;
  const int g = lane >> 4, j = lane & 15;
  const bool roleA = wg < 128;
  const int xc = wg & 7, rk = (wg & 127) >> 3;
  const int h = 4 * xc + (rk >> 2), sub = rk & 3;
  const rsrc_t rs = mkrs(p.ws, WS_BYTES);
  STAMP_DECL
  STAMP(0);

  v4u wqf[18];
  unsigned xs[9], xo[9];
  const int p8 = (roleA ? 0 : 4) + sub;
  if (w == 0) {
    const GAS unsigned* xa = (const GAS unsigned*)((const unsigned char*)p.x + (lane >> 5) * 1152 + (lane & 31) * 36);
    const GAS unsigned* xb = (const GAS unsigned*)((const unsigned char*)p.x + (j >> 3) * 1152 + (4 * p8 + g) * 36);
#pragma unroll
    for (int i = 0; i < 9; ++i) xs[i] = xa[i];
#pragma unroll
    for (int i = 0; i < 9; ++i) xo[i] = xb[i];
    const rsrc_t rq = mkrs(p.wq, 576 * 8192);
    const int vo = g * (18 * 8192) + j * 16, so = p8 * (72 * 8192) + h * 256;
#pragma unroll
    for (int t = 0; t < 18; ++t) wqf[t] = __builtin_amdgcn_raw_buffer_load_b128(rq, vo, so + t * 8192, AUX_NT);
    __builtin_amdgcn_sched_barrier(0);
  }
  unsigned tag;
  asm volatile("s_load_dword %0, %1, 0x0" : "=s"(tag) : "s"(p.ws) : "memory");
  __builtin_amdgcn_s_barrier();
  asm volatile("s_waitcnt lgkmcnt(0)" : "+s"(tag) :: "memory");
  tag += 1u;
  unsigned dm[4];
#pragma unroll
  for (int r = 0; r < 4; ++r) dm[r] = (((j & 7) >> 1) == r) ? ((j & 1) ? 0xFFFF0000u : 0x0000FFFFu) : 0u;
  if (w == 0) {
    float s = 0.f;
#pragma unroll
    for (int i = 0; i < 9; ++i) s = sum2(xs[i], s);
    s = row_sum16(s);
    const float mean0 = (rl(s, 0) + rl(s, 16)) * (1.0f / M), mean1 = (rl(s, 32) + rl(s, 48)) * (1.0f / M);
    const float mm = lane < 32 ? mean0 : mean1, mj = j < 8 ? mean0 : mean1;
    unsigned xd[18];
#pragma unroll
    for (int t = 0; t < 18; ++t) {
      const float xc_ = ((t & 1) ? bf_hi(xo[t >> 1]) : bf_lo(xo[t >> 1])) - mj;
      xd[t] = pk_bf16(xc_, xc_);
    }
    float v = 0.f;
#pragma unroll
    for (int i = 0; i < 9; ++i) {
      const float a = bf_lo(xs[i]) - mm, b = bf_hi(xs[i]) - mm;
      v += a * a;
      v += b * b;
    }
    v = row_sum16(v);
    const float v0 = rl(v, 0) + rl(v, 16), v1 = rl(v, 32) + rl(v, 48);
    const float rj = __builtin_amdgcn_rsqf((j < 8 ? v0 : v1) * (1.0f / M) + 1e-5f);
    f32x4 acc = {0.f, 0.f, 0.f, 0.f};
#pragma unroll
    for (int t = 0; t < 18; ++t) acc = mfma(wqf[t], v4u{xd[t] & dm[0], xd[t] & dm[1], xd[t] & dm[2], xd[t] & dm[3]}, acc);
    const int so = QP_OFF + (h * 8 + p8) * 2048;
    st_sc1(rs, lane * 16, so, v4u{fbits(acc.x * rj), tag, fbits(acc.y * rj), tag});
    st_sc1(rs, lane * 16 + 1024, so, v4u{fbits(acc.z * rj), tag, fbits(acc.w * rj), tag});
    STAMP(1);
  }

  if (roleA) {
    const int cj = sub >> 1, dj = sub & 1;
    if (w >= 4) {
      const rsrc_t ru = mkrs(p.wuk, 512 * 8192);
      const int vo = g * 8192 + ((j ^ (4 * (w - 4) + g)) << 4);
#pragma unroll
      for (int i = 0; i < 8; ++i) {
        const int n = (w - 4) + 4 * i;
        dma(ru, L + A_WUK + n * 1024, vo, (128 * sub + 4 * n) * 8192 + h * 256);
      }
      drain();
    } else if (w == 1) {
      const v4u sv = {SENT, SENT, SENT, SENT};
#pragma unroll
      for (int i = 0; i < 5; ++i)
        if (i < 4 || lane < 32) st_sc1(rs, lane * 16 + i * 1024, OP_OFF + wg * 4608, sv);
      drain();
    }
    bar();
    STAMP(2);
    v4u uk[4];
#pragma unroll
    for (int ks = 0; ks < 4; ++ks) uk[ks] = *(LAS v4u*)(L + A_WUK + (16 * w + j) * 256 + (((4 * ks + g) ^ j) << 4));
    if (w == 2 || w == 3) {
      const int hi = w - 2;
      v4u v[8];
      for (unsigned spin = 0; spin < SPIN_CAP; ++spin) {
        cfence();
        unsigned bad = 0;
#pragma unroll
        for (int i = 0; i < 8; ++i) v[i] = ld_sc1(rs, lane * 16 + (i & 1) * 2048, QP_OFF + (h * 8 + (i & 6)) * 2048 + hi * 1024);
#pragma unroll
        for (int i = 0; i < 8; ++i) bad |= tagdiff(v[i], tag);
        if (__all(bad == 0)) break;
      }
      const float s0 = ((asf(v[0].x) + asf(v[1].x)) + (asf(v[2].x) + asf(v[3].x))) + ((asf(v[4].x) + asf(v[5].x)) + (asf(v[6].x) + asf(v[7].x)));
      const float s1 = ((asf(v[0].z) + asf(v[1].z)) + (asf(v[2].z) + asf(v[3].z))) + ((asf(v[4].z) + asf(v[5].z)) + (asf(v[6].z) + asf(v[7].z)));
      const unsigned pk = pk_bf16(s0, s1);
      LAS u16* qd = (LAS u16*)(L + A_Q) + (j >> 3) * 128 + 8 * (4 * g + 2 * hi) + (j & 7);
      qd[0] = (u16)pk;
      qd[8] = (u16)(pk >> 16);
    }
    bar();
    STAMP(3);
    {
      f32x4 acc = {0.f, 0.f, 0.f, 0.f};
#pragma unroll
      for (int ks = 0; ks < 4; ++ks) acc = mfma(uk[ks], *(LAS v4u*)(L + A_Q + (j & 1) * 256 + ks * 64 + g * 16), acc);
      const float sc = 0.08838834764831845f * 1.4426950408889634f;
      if (j < 2) st_sc1(rs, j * 65536 + g * 16, QA_OFF + h * 2048 + (32 * sub + 4 * w) * 16,
                        v4u{pk_bf16(acc.x * sc, acc.y * sc), tag, pk_bf16(acc.z * sc, acc.w * sc), tag});
    }
    STAMP(4);
    bar();
    float xres = 0.f;
    if (wg < 36 && w == 0) xres = bf_lo(p.x[32 * wg + (lane & 31)]);
    {
      const rsrc_t rv = mkrs(p.wuv, 512 * 8192);
      const int s2 = ((lane >> 3) & 2) | ((w & 1) << 2);
      const int vo = (lane >> 3) * 8192 + (((lane & 7) ^ s2) << 4);
#pragma unroll
      for (int i = 0; i < 4; ++i) {
        const int n = w + 8 * i;
        dma(rv, L + A_WUV + n * 1024, vo, (256 * cj + 8 * n) * 8192 + h * 256 + dj * 128);
      }
      dma_slab(mkrs(p.wo, 4096 * 1152), (h * 128 + 64 * dj) * 1152, L + A_WO, w, lane, cj * 4);
    }
    drain();
    bar();
    STAMP(5);
    v4u wof[5][2];
    v4u uv[8];
    slab_frags(L + A_WO, w, lane, wof);
    if (w < 4) {
      const int b = w >> 1, ch = w & 1;
      {
        const int q = (lane >> 2) & 3, pp = lane & 3;
        const int s = ((q >> 1) & 1) | ((g & 1) << 1);
        LAS unsigned char* b0 = L + A_WUV + (8 * g + q) * 128 + 32 * (w ^ s) + 8 * pp;
#pragma unroll
        for (int ks = 0; ks < 8; ++ks) uv[ks] = tr_frag(b0 + ks * 4096, b0 + ks * 4096 + 512);
      }
      v4u st;
      for (unsigned spin = 0; spin < SPIN_CAP; ++spin) {
        cfence();
        st = ld_sc1(rs, (8 * g + (j & 7)) * 512, ST_OFF + b * 16384 + h * 16);
        if (__all(tagdiff(st, tag) == 0)) break;
      }
      const int pv = g * 4096 + j * 16, ps = PART_OFF + ((h * 2 + cj) * 64 + b * 32) * 512 + ch * 256;
      v4u v[8];
#pragma unroll
      for (int t = 0; t < 8; ++t) v[t] = ld_sc1(rs, pv + t * 512, ps);
      const float ms = asf(st.x), ls = asf(st.z);
      float mx = fmaxf(ms, dpp<0x121>(ms));
      mx = fmaxf(mx, dpp<0x122>(mx));
      mx = fmaxf(mx, dpp<0x124>(mx));
      mx = max_g(mx);
      const float af = __builtin_amdgcn_exp2f(ms - mx);
      const unsigned ad = pk_bf16(af, af);
      float ll = bf_lo(ad) * ls;
      ll += dpp<0x121>(ll);
      ll += dpp<0x122>(ll);
      ll += dpp<0x124>(ll);
      const float inv = __builtin_amdgcn_rcpf(sum_g(ll));
      v4u bw[8];
#define COMB(T) { const unsigned d = (unsigned)__builtin_amdgcn_update_dpp(0, (int)ad, 0x150 + T, 0xf, 0xf, false); \
                  bw[T] = v4u{d & dm[0], d & dm[1], d & dm[2], d & dm[3]}; }
      COMB(0) COMB(1) COMB(2) COMB(3) COMB(4) COMB(5) COMB(6) COMB(7)
#undef COMB
      STAMP(10);
      for (unsigned spin = 0; spin < SPIN_CAP; ++spin) {
        unsigned mxu = 0;
#pragma unroll
        for (int t = 0; t < 8; ++t) mxu = umax(mxu, vmax4(v[t]));
        if (__all(mxu != SENT)) break;
        cfence();
#pragma unroll
        for (int t = 0; t < 8; ++t) v[t] = ld_sc1(rs, pv + t * 512, ps);
      }
      STAMP(6);
      f32x4 acc = {0.f, 0.f, 0.f, 0.f};
#pragma unroll
      for (int t = 0; t < 8; ++t) acc = mfma(v[t], bw[t], acc);
      if (j < 8) {
        LAS u16* cd = (LAS u16*)(L + A_CC) + b * 256 + 128 * ch + 32 * g + 16 * (j >> 2) + (j & 3);
        const unsigned p0 = pk_bf16(acc.x * inv, acc.y * inv), p1 = pk_bf16(acc.z * inv, acc.w * inv);
        cd[0] = (u16)p0; cd[4] = (u16)(p0 >> 16); cd[8] = (u16)p1; cd[12] = (u16)(p1 >> 16);
      }
    } else if (w == 4) {
      for (unsigned spin = 0; spin < SPIN_CAP; ++spin) {
        cfence();
        const v4u st = ld_sc1(rs, lane * 512, ST_OFF + (h ^ 16) * 16);
        if (__all(tagdiff(st, tag) == 0)) break;
      }
    }
    bar();
    if (w < 4) {
      f32x4 acc = {0.f, 0.f, 0.f, 0.f};
#pragma unroll
      for (int ks = 0; ks < 8; ++ks) acc = mfma(uv[ks], *(LAS v4u*)(L + A_CC + (j & 1) * 512 + ks * 64 + g * 16), acc);
      if (j < 2) *(LAS v2u*)(L + A_CTX + j * 128 + (16 * w + 4 * g) * 2) = v2u{pk_bf16(acc.x, acc.y), pk_bf16(acc.z, acc.w)};
    }
    bar();
    slab_gemv(wof, L + A_CTX, rs, OP_OFF + wg * 4608, w, lane);
    STAMP(7);
    if (wg < 36) {
      const float hv = reduce_lines<128>(rs, OP_OFF, wg, L + A_RED, tid, w, lane) + xres;
      if (w == 0) {
        const unsigned pk = pk_bf16(hv, dpp<0xB1>(hv));
        const unsigned pk2 = (unsigned)__builtin_amdgcn_update_dpp(0, (int)pk, 0xAA, 0xf, 0xf, false);
        if (lane < 32 && !(lane & 3)) st_sc1(rs, lane * 4, HB_OFF + wg * 128, v4u{pk, tag, pk2, tag});
      }
      STAMP(8);
      bar();
      const float o = reduce_lines<72>(rs, FP_OFF, wg, L + A_RED, tid, w, lane) + hv;
      if (w == 0) {
        const float other = dpp<0xB1>(o);
        if (lane < 32 && !(lane & 1)) ((GAS unsigned*)p.out)[16 * wg + (lane >> 1)] = pk_bf16(o, other);
        if (wg == 0 && lane == 0) *(GAS unsigned*)p.ws = tag;
      }
      STAMP(9);
    }
  } else {
    const int chunk = xc * 8 + (rk >> 1), hg = rk & 1;
    const int b = chunk >> 5;
    const bool isF = rk < 9;
    const int fj = 9 * xc + rk;
    __builtin_amdgcn_s_barrier();
    {
      const rsrc_t rc = mkrs(p.ckv, 2 * 4096 * 1024);
      const int f0 = ((w & 3) << 2) | (w >> 2);
      const int vo0 = (lane ^ f0) << 4, vo1 = (lane ^ f0 ^ 2) << 4;
#pragma unroll
      for (int i = 0; i < 16; ++i) {
        const int key = (8 * i + w + 64 * hg) & 127;
        dma(rc, L + key * 1024, (i & 1) ? vo1 : vo0, chunk * 131072 + key * 1024);
      }
      const v4u sv = {SENT, SENT, SENT, SENT};
#pragma unroll
      for (int i = 0; i < 2; ++i)
        st_sc1(rs, (lane >> 5) * 32768 + (lane & 31) * 16, PART_OFF + ((16 * hg + 2 * w + i) * 128 + chunk) * 512, sv);
      if (isF && (w < 4 || (w == 4 && lane < 32))) st_sc1(rs, tid * 16, FP_OFF + fj * 4608, sv);
      drain();
    }
    bar();
    STAMP(2);
    v4u kf[16], vf[4][4];
    {
      const int qs = QA_OFF + (b * 32 + 16 * hg) * 2048;
      v4u v[4];
#pragma unroll
      for (int i = 0; i < 4; ++i) v[i] = ld_sc1(rs, tid * 16, qs + i * 8192);
      {
        const int f = ((j & 3) << 2) | ((j >> 2) & 3);
#pragma unroll
        for (int ks = 0; ks < 16; ++ks) kf[ks] = *(LAS v4u*)(L + (16 * w + j) * 1024 + (((4 * ks + g) ^ f) << 4));
        const int q = (lane >> 2) & 3, pp = lane & 3;
#pragma unroll
        for (int T = 0; T < 4; ++T)
#pragma unroll
          for (int ks = 0; ks < 4; ++ks) {
            const int piece = 8 * w + 2 * T + (pp >> 1);
            const int f0 = (q << 2) | (2 * (g & 1)), f1 = f0 | 1;
            LAS unsigned char* rb = L + (32 * ks + 8 * g + q) * 1024 + 8 * (pp & 1);
            vf[T][ks] = tr_frag(rb + ((piece ^ f0) << 4), rb + 4096 + ((piece ^ f1) << 4));
          }
      }
      STAMP(10);
      for (unsigned spin = 0; spin < SPIN_CAP; ++spin) {
        if (__all((tagdiff(v[0], tag) | tagdiff(v[1], tag) | tagdiff(v[2], tag) | tagdiff(v[3], tag)) == 0)) break;
        cfence();
#pragma unroll
        for (int i = 0; i < 4; ++i) v[i] = ld_sc1(rs, tid * 16, qs + i * 8192);
      }
      LAS unsigned char* qd = L + B_QAB + (tid >> 7) * 1040 + (tid & 127) * 8;
#pragma unroll
      for (int i = 0; i < 4; ++i) *(LAS v2u*)(qd + i * 4160) = v2u{v[i].x, v[i].z};
    }
    bar();
    STAMP(3);
    f32x4 sc = {0.f, 0.f, 0.f, 0.f};
    {
      v4u qb[16];
#pragma unroll
      for (int ks = 0; ks < 16; ++ks) qb[ks] = *(LAS v4u*)(L + B_QAB + j * 1040 + ks * 64 + g * 16);
#pragma unroll
      for (int ks = 0; ks < 16; ++ks) sc = mfma(kf[ks], qb[ks], sc);
    }
    float mx = max_g(fmaxf(fmaxf(sc.x, sc.y), fmaxf(sc.z, sc.w)));
    if (lane < 16) *(LAS float*)(L + B_MX + j * 32 + w * 4) = mx;
    bar();
    {
      const f32x4 m0 = *(LAS f32x4*)(L + B_MX + j * 32), m1 = *(LAS f32x4*)(L + B_MX + j * 32 + 16);
      mx = fmaxf(fmaxf(fmaxf(m0.x, m0.y), fmaxf(m0.z, m0.w)), fmaxf(fmaxf(m1.x, m1.y), fmaxf(m1.z, m1.w)));
    }
    const unsigned p0 = pk_bf16(__builtin_amdgcn_exp2f(sc.x - mx), __builtin_amdgcn_exp2f(sc.y - mx));
    const unsigned p1 = pk_bf16(__builtin_amdgcn_exp2f(sc.z - mx), __builtin_amdgcn_exp2f(sc.w - mx));
    *(LAS v2u*)(L + B_PB + (w >> 1) * 1024 + ((2 * (w & 1) + (g >> 1)) * 16 + j) * 16 + 8 * (g & 1)) = v2u{p0, p1};
    bar();
    STAMP(4);
    {
      v4u pb[4];
#pragma unroll
      for (int ks = 0; ks < 4; ++ks) pb[ks] = *(LAS v4u*)(L + B_PB + ks * 1024 + lane * 16);
      if (w == 7) {
        float l0 = 0.f, l1 = 0.f;
#pragma unroll
        for (int ks = 0; ks < 4; ++ks) {
          l0 = sum2(pb[ks].x, l0); l1 = sum2(pb[ks].y, l1); l0 = sum2(pb[ks].z, l0); l1 = sum2(pb[ks].w, l1);
        }
        const float lt = sum_g(l0 + l1);
        if (lane < 16) st_sc1(rs, j * 16, ST_OFF + (chunk * 32 + 16 * hg) * 16, v4u{fbits(mx), tag, fbits(lt), tag});
      }
      f32x4 acc[4];
#pragma unroll
      for (int T = 0; T < 4; ++T) {
        acc[T] = f32x4{0.f, 0.f, 0.f, 0.f};
#pragma unroll
        for (int ks = 0; ks < 4; ++ks) acc[T] = mfma(vf[T][ks], pb[ks], acc[T]);
      }
      const int so = PART_OFF + ((32 * hg + (w >> 2)) * 64 + chunk) * 512 + (w & 3) * 128;
#pragma unroll
      for (int hf = 0; hf < 2; ++hf)
        st_sc1(rs, j * 65536 + g * 16 + hf * 64, so,
               v4u{nosent(pk_bf16(acc[2 * hf].x, acc[2 * hf].y)), nosent(pk_bf16(acc[2 * hf].z, acc[2 * hf].w)),
                   nosent(pk_bf16(acc[2 * hf + 1].x, acc[2 * hf + 1].y)), nosent(pk_bf16(acc[2 * hf + 1].z, acc[2 * hf + 1].w))});
    }
    STAMP(5);
    bar();
    const rsrc_t rg = mkrs(p.wg, 576 * 9216), ru = mkrs(p.wu, 576 * 9216);
    if (!isF) {
      const int hw = (rk - 9) * 8 + w;
#pragma unroll
      for (int i = 0; i < 2; ++i) {
        const int blk = i * 28 + (hw >> 1);
        const int so = (568 - 8 * blk) * 9216 + xc * 1152;
        const rsrc_t rr = (hw & 1) ? ru : rg;
#pragma unroll
        for (int r = 7; r >= 0; --r) dma(rr, L + (w * 18 + i * 9 + r) * 1024, lane * 16, so + r * 9216);
        dma(rr, L + (w * 18 + i * 9 + 8) * 1024, (lane >> 3) * 9216 + (lane & 7) * 16 + 1024, so);
      }
      drain();
    } else {
      dma_slab(mkrs(p.wd, 4608 * 1152), 64 * fj * 1152, L + F_SLAB, w, lane, 0);
      const int sg = ((lane >> 4) & 1) | ((w & 1) << 1);
      const int vg = (lane >> 3) * 9216 + (((lane & 7) ^ sg) << 4), vu = (lane >> 3) * 9216 + (((lane & 7) ^ sg ^ 4) << 4);
#pragma unroll
      for (int i = 0; i < 5; ++i) {
        const int n = w + 8 * i;
        dma(rg, L + F_G + n * 1024, vg, n * 8 * 9216 + fj * 128);
        dma(ru, L + F_U + n * 1024, vu, n * 8 * 9216 + fj * 128);
      }
      if (tid < 72) *(LAS v4u*)(L + F_HB + 2 * F_HBS + tid * 16) = v4u{0x3F803F80u, 0x3F803F80u, 0x3F803F80u, 0x3F803F80u};
      asm volatile("s_waitcnt vmcnt(10)" ::: "memory");
      bar();
      v4u wdf[5][2];
      slab_frags(L + F_SLAB, w, lane, wdf);
      bar();
#pragma unroll
      for (int i = 0; i < 4; ++i) {
        const int n = w + 8 * i;
        dma(rg, L + F_G2 + n * 1024, vg, (n + 40) * 8 * 9216 + fj * 128);
        dma(ru, L + F_U2 + n * 1024, vu, (n + 40) * 8 * 9216 + fj * 128);
      }
      drain();
      bar();
      STAMP(6);
      v4u gu[18];
      {
        const int q = (lane >> 2) & 3, pp = lane & 3, mat = pp >> 1;
        const int s = ((q >> 1) & 1) | ((g & 1) << 1) | (mat << 2);
        const int ro = (8 * g + q) * 128 + ((w ^ s) << 4) + 8 * (pp & 1);
        LAS unsigned char* a1 = L + (mat ? F_U : F_G) + ro;
        LAS unsigned char* a2 = L + (mat ? F_U2 : F_G2) + ro;
#pragma unroll
        for (int ks = 0; ks < 10; ++ks) gu[ks] = tr_frag(a1 + ks * 4096, a1 + ks * 4096 + 512);
#pragma unroll
        for (int ks = 0; ks < 8; ++ks) gu[10 + ks] = tr_frag(a2 + ks * 4096, a2 + ks * 4096 + 512);
      }
      const int l36 = lane < 36 ? lane : 35;
      v4u hv;
      for (unsigned spin = 0; spin < SPIN_CAP; ++spin) {
        cfence();
        hv = ld_sc1(rs, l36 * 16, HB_OFF + w * 576);
        if (__all(tagdiff(hv, tag) == 0)) break;
      }
      STAMP(7);
      *(LAS v2u*)(L + F_HB + (w >> 2) * F_HBS + (w & 3) * 288 + l36 * 8) = v2u{hv.x, hv.z};
      bar();
      v4u hb[18];
#pragma unroll
      for (int ks = 0; ks < 18; ++ks) hb[ks] = *(LAS v4u*)(L + F_HB + (j < 2 ? j : 2) * F_HBS + ks * 64 + g * 16);
      f32x4 acc = {0.f, 0.f, 0.f, 0.f};
#pragma unroll
      for (int ks = 0; ks < 4; ++ks) acc = mfma(gu[ks], hb[ks], acc);
      {
        float s = lane < 36 ? sum2(hv.z, sum2(hv.x, 0.f)) : 0.f;
        s = sum_g(row_sum16(s));
        const float lm = s * (1.0f / 144.0f);
        const float d0 = bf_lo(hv.x) - lm, d1 = bf_hi(hv.x) - lm, d2 = bf_lo(hv.z) - lm, d3 = bf_hi(hv.z) - lm;
        float m2 = lane < 36 ? (d0 * d0 + d1 * d1) + (d2 * d2 + d3 * d3) : 0.f;
        m2 = sum_g(row_sum16(m2));
        if (lane == 0) {
          *(LAS float*)(L + F_ST + (w >> 2) * 32 + (w & 3) * 4) = lm;
          *(LAS float*)(L + F_ST + (w >> 2) * 32 + 16 + (w & 3) * 4) = m2;
        }
      }
#pragma unroll
      for (int ks = 4; ks < 10; ++ks) acc = mfma(gu[ks], hb[ks], acc);
      bar();
      const f32x4 a0 = *(LAS f32x4*)(L + F_ST + (j & 1) * 32), c0 = *(LAS f32x4*)(L + F_ST + (j & 1) * 32 + 16);
#pragma unroll
      for (int ks = 10; ks < 18; ++ks) acc = mfma(gu[ks], hb[ks], acc);
      const float mean = ((a0.x + a0.y) + (a0.z + a0.w)) * 0.25f;
      const f32x4 dd = a0 - mean;
      const float between = (dd.x * dd.x + dd.y * dd.y) + (dd.z * dd.z + dd.w * dd.w);
      const float within = (c0.x + c0.y) + (c0.z + c0.w);
      const float rstd = __builtin_amdgcn_rsqf((within + 144.0f * between) * (1.0f / M) + 1e-5f);
      float pre[4];
#pragma unroll
      for (int r = 0; r < 4; ++r) pre[r] = (acc[r] - mean * dpp<0xAA>(acc[r])) * rstd;
      {
        auto s0 = __builtin_amdgcn_permlane32_swap(fbits(pre[0]), fbits(pre[2]), false, false);
        auto s1 = __builtin_amdgcn_permlane32_swap(fbits(pre[1]), fbits(pre[3]), false, false);
        const unsigned g0 = s0[0], u0 = s0[1], g1 = s1[0], u1 = s1[1];
        const float ga = asf(g0), gb = asf(g1);
        const float aa = ga * __builtin_amdgcn_rcpf(1.0f + __builtin_amdgcn_exp2f(-1.4426950408889634f * ga)) * asf(u0);
        const float ab = gb * __builtin_amdgcn_rcpf(1.0f + __builtin_amdgcn_exp2f(-1.4426950408889634f * gb)) * asf(u1);
        if (j < 2) *(LAS unsigned*)(L + F_ACT + j * 128 + (8 * w + 4 * (g & 1) + 2 * (g >> 1)) * 2) = pk_bf16(aa, ab);
      }
      bar();
      slab_gemv(wdf, L + F_ACT, rs, FP_OFF + fj * 4608, w, lane);
      STAMP(8);
    }
  }
  STAMP_FLUSH;
}
}

extern "C" void kernel_launch(void* const* d_in, const int* in_sizes, int n_in, void* d_out, int out_size, void* d_ws,
                              size_t ws_size, hipStream_t stream) {
  Params p;
  p.x = (const u16*)d_in[0];
  p.ckv = (const u16*)d_in[1];
  p.wq = (const u16*)d_in[2];
  p.wuk = (const u16*)d_in[3];
  p.wuv = (const u16*)d_in[4];
  p.wo = (const u16*)d_in[5];
  p.wg = (const u16*)d_in[7];
  p.wu = (const u16*)d_in[8];
  p.wd = (const u16*)d_in[9];
  p.out = (u16*)d_out;
  p.ws = (unsigned char*)d_ws;
  (void)hipFuncSetAttribute((const void*)fused_kernel, hipFuncAttributeMaxDynamicSharedMemorySize, LDS_BYTES);
  hipLaunchKernelGGL(fused_kernel, dim3(256), dim3(NTHR), LDS_BYTES, stream, p);
}
```

```cpp
#include <hip/hip_runtime.h>
#include <hip/hip_bf16.h>
#include <stdint.h>

namespace {
#define GAS __attribute__((address_space(1)))
#define LAS __attribute__((address_space(3)))
typedef unsigned short u16;
typedef unsigned v4u __attribute__((ext_vector_type(4)));
typedef int v4i __attribute__((ext_vector_type(4)));
typedef unsigned v2u __attribute__((ext_vector_type(2)));
typedef float f32x4 __attribute__((ext_vector_type(4)));
typedef float f32x2 __attribute__((ext_vector_type(2)));
typedef short s16x4 __attribute__((ext_vector_type(4)));
typedef __bf16 bf16x8 __attribute__((ext_vector_type(8)));
typedef __bf16 bf16x2 __attribute__((ext_vector_type(2)));
typedef __amdgpu_buffer_rsrc_t rsrc_t;

constexpr int NTHR = 512;
constexpr int M = 576;
constexpr int OF_OFF = 4096;
constexpr int TOK_OFF = OF_OFF + 64 * 128;
constexpr int QP_OFF = TOK_OFF + 128 * 128;
constexpr int QA_OFF = QP_OFF + 32 * 6 * 2048;
constexpr int ST_OFF = QA_OFF + 2 * 32 * 2048;
constexpr int PART_OFF = ST_OFF + 64 * 32 * 16;
constexpr int OP_OFF = PART_OFF + 32 * 64 * 1024;
constexpr int HB_OFF = OP_OFF + 64 * 9216;
constexpr int HS_OFF = HB_OFF + 128;
constexpr int FP_OFF = HB_OFF + 36 * 256;
constexpr int WS_BYTES = FP_OFF + 72 * 9216;
constexpr int K_QAB = 131072, K_MX = K_QAB + 16 * 1040, K_PB = K_MX + 512, K_END = K_PB + 4096;
constexpr int F_SLAB = 0, F_G1 = 73728, F_U1 = F_G1 + 24576;
constexpr int F_G2 = F_U1 + 24576, F_U2 = F_G2 + 16384;
constexpr int F_G3 = 0, F_U3 = 32768;
constexpr int F_HB = F_U2 + 16384, F_HBS = 1184, F_ST = F_HB + 3 * F_HBS, F_ACT = F_ST + 128, F_END = F_ACT + 384;
constexpr int Q_WUK = 0, Q_Q = 65536;
constexpr int O_WUV = 0, O_WO = 65536, O_CC = O_WO + 73728, O_CTX = O_CC + 2176, O_RED = O_CTX + 384, O_ST = O_RED + 1024, O_SCR = 147456;
constexpr int X_Q1 = F_END, X_SCR = X_Q1 + 1024 + 16, LDS_BYTES = X_SCR + 256;
static_assert(K_END <= X_Q1 && O_SCR + 8192 <= X_Q1 && LDS_BYTES <= 163840, "LDS");
constexpr unsigned SENT = 0xFFFFFFFFu;
constexpr unsigned SPIN_CAP = 1u << 20;
constexpr int AUX_SC1 = 16, AUX_NT = 2;
constexpr int CODE_BYTES = 22528;
constexpr int PSLEEP = 2;
constexpr int TK = 8, TS = 8;
constexpr int QD = 2, KD = 1;
constexpr int D0 = 275, D1 = 450;
constexpr int ODELAY = 250, FDELAY = 470;
constexpr int S1 = 112, S2 = 130;
constexpr int D3 = 70;
constexpr int D5 = 13;
constexpr int PRE = 4;

struct Params {
  const u16 *x, *wq, *wuk, *ckv, *wuv, *wo, *wg, *wu, *wd;
  u16* out;
  unsigned char* ws;
};
static_assert(sizeof(Params) == 88, "no padding");

#define DEV __device__ __forceinline__
#define SCHED() __builtin_amdgcn_sched_barrier(0)
DEV float bf_lo(unsigned u) { return __builtin_bit_cast(float, u << 16); }
DEV float bf_hi(unsigned u) { return __builtin_bit_cast(float, u & 0xFFFF0000u); }
DEV unsigned fbits(float f) { return __builtin_bit_cast(unsigned, f); }
DEV float asf(unsigned u) { return __builtin_bit_cast(float, u); }
DEV float mx3(float a, float b, float c) { return __builtin_fmaxf(__builtin_fmaxf(a, b), c); }
DEV unsigned pk_bf16(float a, float b) {
  f32x2 v = {a, b};
  return __builtin_bit_cast(unsigned, __builtin_convertvector(v, bf16x2));
}
DEV float sum2(unsigned pair, float acc) {
  return __builtin_amdgcn_fdot2_f32_bf16(__builtin_bit_cast(bf16x2, pair), __builtin_bit_cast(bf16x2, 0x3F803F80u), acc, false);
}
DEV f32x4 mfma(v4u a, v4u b, f32x4 c) {
  return __builtin_amdgcn_mfma_f32_16x16x32_bf16(__builtin_bit_cast(bf16x8, a), __builtin_bit_cast(bf16x8, b), c, 0, 0, 0);
}
DEV rsrc_t mkrs(const void* p, int bytes) { return __builtin_amdgcn_make_buffer_rsrc((void*)p, 0, bytes, 0x00020000); }
DEV v4i mkdesc(const void* p, int bytes) {
  const unsigned long long a = (unsigned long long)p;
  return v4i{(int)(unsigned)a, (int)(unsigned)(a >> 32) & 0xffff, bytes, 0x00020000};
}
DEV void dma(v4i d, int lds, int voff, int soff) {
  asm volatile("s_mov_b32 m0, %0\n\ts_nop 0\n\tbuffer_load_dwordx4 %1, %2, %3 offen nt lds" ::"s"(lds), "v"(voff), "s"(d), "s"(soff) : "memory");
}
template <int N> DEV void throttle() { asm volatile("s_waitcnt vmcnt(%0)" ::"i"(N) : "memory"); }
DEV void dma_keep(v4i d, int lds, int voff, int soff) {
  asm volatile("s_mov_b32 m0, %0\n\ts_nop 0\n\tbuffer_load_dwordx4 %1, %2, %3 offen lds" ::"s"(lds), "v"(voff), "s"(d), "s"(soff) : "memory");
}
DEV v4u ld_sc1(rsrc_t rs, int voff, int soff) { return __builtin_amdgcn_raw_buffer_load_b128(rs, voff, soff, AUX_SC1); }
DEV void st_sc1(rsrc_t rs, int voff, int soff, v4u v) { __builtin_amdgcn_raw_buffer_store_b128(v, rs, voff + soff, 0, AUX_SC1); }
DEV void bar() { asm volatile("s_waitcnt lgkmcnt(0)\n\ts_barrier" ::: "memory"); }
DEV void drain() { asm volatile("s_waitcnt vmcnt(0)" ::: "memory"); }
DEV void cfence() { asm volatile("" ::: "memory"); }
template <int CTRL> DEV float dpp(float v) {
  return __builtin_bit_cast(float, __builtin_amdgcn_update_dpp(0, __builtin_bit_cast(int, v), CTRL, 0xf, 0xf, false));
}
DEV float row_sum16(float v) { v += dpp<0x128>(v); v += dpp<0x124>(v); v += dpp<0x122>(v); v += dpp<0x121>(v); return v; }
DEV float rl(float v, int l) { return __builtin_bit_cast(float, __builtin_amdgcn_readlane(__builtin_bit_cast(int, v), l)); }
DEV void x16(float v, float& a, float& b) {
  auto r = __builtin_amdgcn_permlane16_swap(fbits(v), fbits(v), false, false);
  const unsigned r0 = r[0], r1 = r[1];
  a = asf(r0); b = asf(r1);
}
DEV void x32(float v, float& a, float& b) {
  auto r = __builtin_amdgcn_permlane32_swap(fbits(v), fbits(v), false, false);
  const unsigned r0 = r[0], r1 = r[1];
  a = asf(r0); b = asf(r1);
}
DEV float sum_g(float v) { float a, b; x16(v, a, b); v = a + b; x32(v, a, b); return a + b; }
DEV float max_g(float v, float any) { float a, b; x16(v, a, b); v = mx3(a, b, any); x32(v, a, b); return mx3(a, b, any); }
DEV v4u tr_frag(LAS unsigned char* a0, LAS unsigned char* a1) {
  const s16x4 lo = __builtin_amdgcn_ds_read_tr16_b64_v4i16((LAS s16x4*)a0);
  const s16x4 hi = __builtin_amdgcn_ds_read_tr16_b64_v4i16((LAS s16x4*)a1);
  const v2u l2 = __builtin_bit_cast(v2u, lo), h2 = __builtin_bit_cast(v2u, hi);
  return v4u{l2.x, l2.y, h2.x, h2.y};
}
DEV unsigned umin(unsigned a, unsigned b) { return a < b ? a : b; }
DEV unsigned umax(unsigned a, unsigned b) { return a > b ? a : b; }
DEV unsigned nosent(unsigned u) { return umin(u, 0xFFFFFFFEu); }
DEV unsigned vmax4(v4u v, unsigned m) { return umax(umax(umax(v.x, v.y), m), umax(v.z, v.w)); }
DEV unsigned tagdiff(v4u v, unsigned tag) { return (v.y ^ tag) | (v.w ^ tag); }
DEV int since(unsigned t) { return (int)((unsigned)__builtin_amdgcn_s_memrealtime() - t); }
DEV bool anybad(bool b) { return __builtin_amdgcn_ballot_w64(b) != 0ull; }

#define RETRY_TAGGED(N, v, LOAD) { \
    unsigned spin_ = 0; \
    _Pragma("unroll 1") \
    for (;;) { \
      unsigned bad_ = 0; \
      _Pragma("unroll") \
      for (int i = 0; i < (N); ++i) bad_ |= anybad(tagdiff(v[i], tag) != 0) ? (1u << i) : 0u; \
      if (bad_ == 0 || ++spin_ >= SPIN_CAP) break; \
      cfence(); \
      _Pragma("unroll") \
      for (int i = 0; i < (N); ++i) \
        if (bad_ & (1u << i)) { LOAD(i); } \
    } }

template <int T> DEV void dma_slab(v4i d, int sbase, int dst, int w, int lane, int rot) {
#pragma unroll 1
  for (int i = 0; i < 9; ++i) {
    int ii = i + rot;
    ii = ii >= 9 ? ii - 9 : ii;
    const int n = w + 8 * ii;
    const unsigned P = 64 * n + lane, row = (P * 58255u) >> 22;
    const unsigned s2 = (row & 2u) | ((row >> 1) & 4u);
    dma(d, dst + n * 1024, (int)((P ^ s2) << 4), sbase);
    throttle<T>();
  }
}
template <int I0, int N> DEV void slab_frags(LAS unsigned char* base, int w4, int lane, v4u (&a)[N][2]) {
  const int g = lane >> 4, q = (lane >> 2) & 3, pp = lane & 3;
  const int s = ((q >> 1) & 1) | ((g & 1) << 1);
  LAS unsigned char* b0 = base + (8 * g + q) * 1152 + 32 * (w4 ^ s) + 8 * pp + 128 * I0;
#pragma unroll
  for (int i = 0; i < N; ++i)
#pragma unroll
    for (int ks = 0; ks < 2; ++ks) a[i][ks] = tr_frag(b0 + 128 * i + ks * 32 * 1152, b0 + 128 * i + ks * 32 * 1152 + 4 * 1152);
}
template <int I0, int N> DEV void slab_gemv(const v4u (&a)[N][2], LAS unsigned char* vec, rsrc_t rs, int obase, int w4, int lane, unsigned tag) {
  const int g = lane >> 4, j = lane & 15;
  LAS unsigned char* vp = vec + (j & 1) * 192 + 16 * g;
  const int o = (j & 1) * 4608 + 16 * g + (j >> 1) * 512 + I0 * 512, so = obase + 128 * w4;
  bar();
  const v4u b0 = *(LAS v4u*)vp, b1 = *(LAS v4u*)(vp + 64);
  f32x4 sel = {0.f, 0.f, 0.f, 0.f};
#pragma unroll
  for (int i = 0; i < N; ++i) {
    f32x4 acc = mfma(a[i][0], b0, f32x4{0.f, 0.f, 0.f, 0.f});
    acc = mfma(a[i][1], b1, acc);
    sel = (i == 0 || (j >> 1) == i) ? acc : sel;
  }
  if (N == 8 || j < 2 * N) {
    st_sc1(rs, o, so, v4u{fbits(sel.x), tag, fbits(sel.y), tag});
    st_sc1(rs, o + 64, so, v4u{fbits(sel.z), tag, fbits(sel.w), tag});
  }
}
template <int NP>
DEV float reduce_lines(rsrc_t rs, int base, int rr, LAS unsigned char* red, int tid, int w, int lane, unsigned tag) {
  const int pc = tid & 15, pr = tid >> 4;
  const bool three = pr + 64 < NP;
  const int o0 = pr * 9216 + pc * 16, o2 = three ? o0 + 64 * 9216 : o0;
  LAS float* dst = (LAS float*)(red + w * 128 + (16 * (pc >> 3) + 4 * (pc & 3) + 2 * ((pc >> 2) & 1) + (lane >> 5)) * 4);
  constexpr int NL = NP > 64 ? 3 : 2;
  v4u v[NL];
#define LD_RED(i) v[i] = ld_sc1(rs, (i) == 2 ? o2 : o0, base + rr * 256 + ((i) == 1 ? 32 * 9216 : 0))
#pragma unroll
  for (int i = 0; i < NL; ++i) LD_RED(i);
  RETRY_TAGGED(NL, v, LD_RED)
  float sx = asf(v[0].x) + asf(v[1].x), sz = asf(v[0].z) + asf(v[1].z);
  if (NP > 64 && three) { sx += asf(v[NL - 1].x); sz += asf(v[NL - 1].z); }
  {
    auto r01 = __builtin_amdgcn_permlane32_swap(fbits(sx), fbits(sz), false, false);
    const unsigned a0 = r01[0], a1 = r01[1];
    float ta, tb;
    x16(asf(a0) + asf(a1), ta, tb);
    if ((lane & 16) == 0) *dst = ta + tb;
  }
  bar();
  float r = 0.f;
  if (w == 0) {
    float t[8];
#pragma unroll
    for (int ww = 0; ww < 8; ++ww) t[ww] = *(LAS float*)(red + ww * 128 + (lane & 31) * 4);
    r = ((t[0] + t[1]) + (t[2] + t[3])) + ((t[4] + t[5]) + (t[6] + t[7]));
  }
  return r;
}

__global__ void __launch_bounds__(NTHR, 2) fused_kernel(Params p) {
  extern __shared__ __attribute__((aligned(16))) unsigned char smem[];
  LAS unsigned char* L = (LAS unsigned char*)smem;
  const int LB = (int)(unsigned)(size_t)L;
  const int tid = threadIdx.x, lane = tid & 63, w = __builtin_amdgcn_readfirstlane(tid >> 6), wg = blockIdx.x;
  const int g = lane >> 4, j = lane & 15, w4 = w & 3;
  const int xc = wg & 7, r5 = wg >> 3;
  const bool roleQ = r5 < 8, roleK = r5 >= 8 && r5 < 24;
  const int rk = r5 - 8, r3 = r5 & 7;
  const int h = 4 * xc + (r3 >> 1), hb = r3 & 1;
  const int chunk = xc * 8 + (rk >> 1), hg = rk & 1;
  const bool isF = roleQ || rk == 0;
  const int i9 = (3 - xc - (int)((unsigned)(size_t)p.wg >> 7)) & 7;
  const int fj = 9 * xc + (roleQ ? (r5 == i9 ? 8 : r5) : i9);
  const int oi = xc + 8 * r3;
  const rsrc_t rs = mkrs(p.ws, WS_BYTES);
  unsigned long long pc0;
  asm volatile("s_getpc_b64 %0" : "=s"(pc0));
  const unsigned tstart = (unsigned)__builtin_amdgcn_s_memrealtime();

  unsigned tag;
  const v4i dc = mkdesc(p.ckv, 2 * 4096 * 1024);
  unsigned dm[4];
#pragma unroll
  for (int r = 0; r < 4; ++r) dm[r] = (((j & 7) >> 1) == r) ? ((j & 1) ? 0xFFFF0000u : 0x0000FFFFu) : 0u;
  float xres = 0.f, xmean = 0.f;
#define CKV_ROW(i) { const int r_ = 4 * (i) + w4; \
    dma_keep(dc, LB + r_ * 1024, (lane ^ (((r_ & 7) << 1) | ((r_ >> 3) & 1))) << 4, chunk * 131072 + ((r_ + 64 * hg) & 127) * 1024); }
#define GET_TAG() do { asm volatile("s_load_dword %0, %1, 0x0\n\ts_waitcnt lgkmcnt(0)" : "=s"(tag) : "s"(p.ws) : "memory"); tag = tag * 1664525u + 1013904223u; } while (0)
#define RAWBAR() do { SCHED(); asm volatile("s_barrier" ::: "memory"); SCHED(); } while (0)
#define CODE_WARM() { \
    const v4i dcode = mkdesc((const void*)(pc0 & ~63ull), CODE_BYTES); \
    const int co_ = (int)umin(2048u + (unsigned)(r5 % 6) * 4096u + (unsigned)lane * 64u, (unsigned)CODE_BYTES - 4u); \
    asm volatile("s_mov_b32 m0, %0\n\ts_nop 0\n\tbuffer_load_dword %1, %2, 0 offen lds" ::"s"(LB + X_SCR), "v"(co_), "s"(dcode) : "memory"); }

  const int h1 = 4 * xc + (rk & 3), p6 = rk >> 2;
  const GAS v4u* xa = (const GAS v4u*)((const unsigned char*)p.x + (lane >> 5) * 1152) + (lane & 31);
#define LOAD_XS() v4u xq0 = xa[0], xq1 = xa[32], xq2 = xa[(lane & 31) < 8 ? 64 : 0];
#define UNPACK_XS() const bool x3 = (lane & 31) < 8; const unsigned xs[12] = {xq0.x, xq0.y, xq0.z, xq0.w, xq1.x, xq1.y, xq1.z, xq1.w, xq2.x, xq2.y, xq2.z, xq2.w};
#define SUM_XS(s) float s = 0.f; { float s3 = 0.f; _Pragma("unroll") for (int i = 0; i < 8; ++i) s = sum2(xs[i], s); _Pragma("unroll") for (int i = 8; i < 12; ++i) s3 = sum2(xs[i], s3); s += x3 ? s3 : 0.f; }
  const int kind = roleQ ? 0 : (w < 2 ? 1 : (w == 2 ? 3 : 2));
  volatile LAS unsigned* xflag = (volatile LAS unsigned*)(L + X_Q1 + 1024 + 8);
  if (kind >= 2) {
    if (kind == 3) {
      LOAD_XS()
      RAWBAR();
      GET_TAG();
      UNPACK_XS()
      SUM_XS(s)
      s = row_sum16(s);
      const float mm = (lane < 32 ? rl(s, 0) + rl(s, 16) : rl(s, 32) + rl(s, 48)) * (1.0f / M);
      float v = 0.f, v3 = 0.f;
#pragma unroll
      for (int i = 0; i < 12; ++i) {
        const float a = bf_lo(xs[i]) - mm, b = bf_hi(xs[i]) - mm;
        if (i < 8) { v += a * a; v += b * b; } else { v3 += a * a; v3 += b * b; }
      }
      v = row_sum16(v + (x3 ? v3 : 0.f));
      float va, vb;
      x16(v, va, vb);
      if ((lane & 31) == 0) *(LAS float*)(L + X_Q1 + 1024 + (lane >> 5) * 4) = __builtin_amdgcn_rsqf((va + vb) * (1.0f / M) + 1e-5f);
      cfence();
      xflag[1] = 1u;
    } else {
      RAWBAR();
      if (w == 3) { CODE_WARM() }
      else if (roleK) {
        for (int i = 0; i < KD; ++i) __builtin_amdgcn_s_sleep(4);
#pragma unroll 1
        for (int i = 0; i < 32; ++i) {
          CKV_ROW(i)
          throttle<TK>();
          if (i == 15 + TK) RAWBAR();
        }
      }
      GET_TAG();
    }
  } else if (kind == 1) {
    v4u wqf[12];
    LOAD_XS()
    const int row0 = 96 * p6 + 48 * w;
    const unsigned char* xb = (const unsigned char*)p.x + (j >> 3) * 1152 + row0 * 2;
    const v4u xo0 = *(const GAS v4u*)(xb + 16 * g);
    const v2u xo1 = *(const GAS v2u*)(xb + 64 + 8 * g);
    const rsrc_t rq = mkrs(p.wq, 576 * 8192);
    const int vo0 = g * (8 * 8192) + j * 16, vo1 = g * (4 * 8192) + j * 16, so = row0 * 8192 + h1 * 256;
#pragma unroll
    for (int t = 0; t < 12; ++t)
      wqf[t] = __builtin_amdgcn_raw_buffer_load_b128(rq, t < 8 ? vo0 : vo1, so + (t < 8 ? t : 24 + t) * 8192, AUX_NT);
    const unsigned xr = p.x[32 * (oi < 36 ? oi : 0) + (lane & 31)];
    if (tid == 0) { xflag[0] = 0u; xflag[1] = 0u; }
    asm volatile("s_waitcnt lgkmcnt(0)" ::: "memory");
    RAWBAR();
    GET_TAG();
    UNPACK_XS()
    SUM_XS(s)
    s = row_sum16(s);
    const unsigned xo[6] = {xo0.x, xo0.y, xo0.z, xo0.w, xo1.x, xo1.y};
    const float m0 = rl(s, 0) + rl(s, 16), m1 = rl(s, 32) + rl(s, 48);
    const float mj = (j < 8 ? m0 : m1) * (1.0f / M);
    f32x4 acc = {0.f, 0.f, 0.f, 0.f};
#pragma unroll
    for (int t = 0; t < 12; ++t) {
      const float xc_ = ((t & 1) ? bf_hi(xo[t >> 1]) : bf_lo(xo[t >> 1])) - mj;
      const unsigned d = pk_bf16(xc_, xc_);
      acc = mfma(wqf[t], v4u{d & dm[0], d & dm[1], d & dm[2], d & dm[3]}, acc);
    }
    xmean = (oi < 18 ? m0 : m1) * (1.0f / M);
    xres = bf_lo(xr) - xmean;
    asm volatile("" : "+v"(xres));
    if (w == 1) {
      *(LAS f32x4*)(L + X_Q1 + lane * 16) = acc;
      cfence();
      xflag[0] = 1u;
    } else {
      f32x4 o;
      float rj;
      unsigned spin = 0, f0, f1;
#pragma unroll 1
      do {
        cfence();
        f0 = xflag[0];
        f1 = xflag[1];
        cfence();
        o = *(LAS f32x4*)(L + X_Q1 + lane * 16);
        rj = *(LAS float*)(L + X_Q1 + 1024 + (j >> 3) * 4);
      } while (anybad((f0 & f1) == 0) && ++spin < SPIN_CAP);
      acc = (acc + o) * rj;
      const int sq = QP_OFF + (h1 * 6 + p6) * 2048;
      st_sc1(rs, lane * 16, sq, v4u{fbits(acc.x), tag, fbits(acc.y), tag});
      st_sc1(rs, lane * 16 + 1024, sq, v4u{fbits(acc.z), tag, fbits(acc.w), tag});
    }
  } else {
    const v4i dk = mkdesc(p.wuk, 512 * 8192);
    const int vo = g * 8192 + ((j ^ (4 * w4 + g)) << 4);
    for (int i = 0; i < QD; ++i) __builtin_amdgcn_s_sleep(4);
#pragma unroll 1
    for (int i = 0; i < 8; ++i) {
      const int n = w + 8 * i;
      dma(dk, LB + Q_WUK + n * 1024, vo, (256 * hb + 4 * n) * 8192 + h * 256);
    }
    GET_TAG();
  }

  const v4i dg = mkdesc(p.wg, 576 * 9216), du = mkdesc(p.wu, 576 * 9216), dd = mkdesc(p.wd, 4608 * 1152);
  const int sg = ((lane >> 3) & 2) | ((w & 1) << 2);
  if (roleQ) {
    drain();
    bar();
    if (w >= 4) {
      v4u uk[4][4];
#pragma unroll
      for (int t = 0; t < 4; ++t)
#pragma unroll
        for (int ks = 0; ks < 4; ++ks) uk[t][ks] = *(LAS v4u*)(L + Q_WUK + (64 * w4 + 16 * t + j) * 256 + (((4 * ks + g) ^ j) << 4));
      const int qo = (j & 1) * 65536 + ((j >> 1) & 3) * 64 + g * 16, qso = QA_OFF + h * 2048 + (64 * hb + 16 * w4) * 16;
      bar();
      v4u qb[4];
#pragma unroll
      for (int ks = 0; ks < 4; ++ks) qb[ks] = *(LAS v4u*)(L + Q_Q + (j & 1) * 320 + ks * 64 + g * 16);
      SCHED();
      f32x4 sel = {0.f, 0.f, 0.f, 0.f};
#pragma unroll
      for (int t = 0; t < 4; ++t) {
        f32x4 a = {0.f, 0.f, 0.f, 0.f};
#pragma unroll
        for (int ks = 0; ks < 4; ++ks) a = mfma(uk[t][ks], qb[ks], a);
        sel = (t == 0 || ((j >> 1) & 3) == t) ? a : sel;
      }
      const float sc = 0.08838834764831845f * 1.4426950408889634f;
      sel *= sc;
      if (j < 8) st_sc1(rs, qo, qso, v4u{pk_bf16(sel.x, sel.y), tag, pk_bf16(sel.z, sel.w), tag});
    } else {
      if (w < 2) {
        while (since(tstart) < D0) __builtin_amdgcn_s_sleep(1);
        v4u v[6];
#define LD_QP(i) v[i] = ld_sc1(rs, lane * 16 + ((i) & 1) * 2048, QP_OFF + (h * 6 + ((i) & 6)) * 2048 + w * 1024)
#pragma unroll
        for (int i = 0; i < 6; ++i) LD_QP(i);
        RETRY_TAGGED(6, v, LD_QP)
        const float s0 = ((asf(v[0].x) + asf(v[1].x)) + (asf(v[2].x) + asf(v[3].x))) + (asf(v[4].x) + asf(v[5].x));
        const float s1 = ((asf(v[0].z) + asf(v[1].z)) + (asf(v[2].z) + asf(v[3].z))) + (asf(v[4].z) + asf(v[5].z));
        const unsigned pk = pk_bf16(s0, s1);
        LAS u16* qd = (LAS u16*)(L + Q_Q) + (j >> 3) * 160 + 8 * (4 * g + 2 * w) + (j & 7);
        qd[0] = (u16)pk;
        qd[8] = (u16)(pk >> 16);
      }
      bar();
    }
    bar();
  } else if (roleK) {
    const int b = chunk >> 5;
    const int qs = QA_OFF + (b * 32 + 16 * hg) * 2048;
    LAS unsigned char* qd = L + K_QAB + (tid >> 7) * 1040 + (tid & 127) * 8;
#define LD_QA(i) v[i] = ld_sc1(rs, tid * 16, qs + (i) * 8192)
#define GATHER_QABS() { \
      RETRY_TAGGED(4, v, LD_QA) \
      _Pragma("unroll") for (int i = 0; i < 4; ++i) *(LAS v2u*)(qd + i * 4160) = v2u{v[i].x, v[i].z}; }
    if (w < 4) {
      v4u kf[2][16];
      const int f = ((j & 7) << 1) | (j >> 3);
      bar();
#pragma unroll
      for (int ks = 0; ks < 16; ++ks) kf[0][ks] = *(LAS v4u*)(L + (16 * w + j) * 1024 + (((4 * ks + g) ^ f) << 4));
      bar();
#pragma unroll
      for (int ks = 0; ks < 16; ++ks) kf[1][ks] = *(LAS v4u*)(L + (64 + 16 * w + j) * 1024 + (((4 * ks + g) ^ f) << 4));
      SCHED();
      while (since(tstart) < D1) __builtin_amdgcn_s_sleep(1);
      v4u v[4];
#pragma unroll
      for (int i = 0; i < 4; ++i) v[i] = ld_sc1(rs, tid * 16, qs + i * 8192);
      GATHER_QABS()
      bar();
      f32x4 s0 = {0.f, 0.f, 0.f, 0.f}, s1 = {0.f, 0.f, 0.f, 0.f};
      {
        v4u qb[16];
#pragma unroll
        for (int ks = 0; ks < PRE; ++ks) qb[ks] = *(LAS v4u*)(L + K_QAB + j * 1040 + ks * 64 + g * 16);
        SCHED();
#pragma unroll
        for (int ks = 0; ks < 16; ++ks) {
          s0 = mfma(kf[0][ks], qb[ks], s0);
          if (ks + PRE < 16) qb[ks + PRE] = *(LAS v4u*)(L + K_QAB + j * 1040 + (ks + PRE) * 64 + g * 16);
          s1 = mfma(kf[1][ks], qb[ks], s1);
          SCHED();
        }
      }
      float mx = max_g(mx3(mx3(mx3(s0.x, s0.y, s0.z), s0.w, s1.x), mx3(s1.y, s1.z, s1.w), s0.x), s0.x);
      if (lane < 16) *(LAS float*)(L + K_MX + j * 16 + w * 4) = mx;
      bar();
      {
        const f32x4 m0 = *(LAS f32x4*)(L + K_MX + j * 16);
        mx = mx3(mx3(m0.x, m0.y, m0.z), m0.w, mx);
      }
      v4u pf;
      pf.x = pk_bf16(__builtin_amdgcn_exp2f(s0.x - mx), __builtin_amdgcn_exp2f(s0.y - mx));
      pf.y = pk_bf16(__builtin_amdgcn_exp2f(s0.z - mx), __builtin_amdgcn_exp2f(s0.w - mx));
      pf.z = pk_bf16(__builtin_amdgcn_exp2f(s1.x - mx), __builtin_amdgcn_exp2f(s1.y - mx));
      pf.w = pk_bf16(__builtin_amdgcn_exp2f(s1.z - mx), __builtin_amdgcn_exp2f(s1.w - mx));
      *(LAS v4u*)(L + K_PB + w * 1024 + lane * 16) = pf;
      {
        const float l8 = ((bf_lo(pf.x) + bf_hi(pf.x)) + (bf_lo(pf.y) + bf_hi(pf.y))) + ((bf_lo(pf.z) + bf_hi(pf.z)) + (bf_lo(pf.w) + bf_hi(pf.w)));
        *(LAS float*)(L + K_QAB + j * 80 + (w * 4 + g) * 4) = l8;
      }
      bar();
      if (w == 0) {
        const f32x4 l4 = *(LAS f32x4*)(L + K_QAB + j * 80 + g * 16);
        const float lt = sum_g((l4.x + l4.y) + (l4.z + l4.w));
        if (lane < 16) st_sc1(rs, j * 16, ST_OFF + (chunk * 32 + 16 * hg) * 16, v4u{fbits(mx), tag, fbits(lt), tag});
      }
    } else {
      {
        const v4u sv = {SENT, SENT, SENT, SENT};
#pragma unroll
        for (int i = 0; i < 4; ++i) st_sc1(rs, g * 65536 + j * 16, PART_OFF + ((16 * hg + 4 * i) * 64 + chunk) * 1024 + w4 * 256, sv);
      }
      v2u vf[8][4][2];
      const int q = (lane >> 2) & 3, pp = lane & 3;
      const int f = (((4 * g + q) & 7) << 1) | (g >> 1);
      LAS unsigned char* rb = L + (4 * g + q) * 1024 + w4 * 256 + 8 * (pp & 1);
#pragma unroll
      for (int T = 0; T < 8; ++T)
#pragma unroll
        for (int ks = 0; ks < 4; ++ks)
          vf[T][ks][0] = __builtin_bit_cast(v2u, __builtin_amdgcn_ds_read_tr16_b64_v4i16((LAS s16x4*)(rb + ks * 16384 + (((2 * T + (pp >> 1)) ^ f) << 4))));
      throttle<4>();
      bar();
      while (since(tstart) < D1) __builtin_amdgcn_s_sleep(1);
      v4u v[4];
#pragma unroll
      for (int i = 0; i < 4; ++i) v[i] = ld_sc1(rs, tid * 16, qs + i * 8192);
      GATHER_QABS()
      drain();
      bar();
      if (tid == 256) {
        const unsigned now = (unsigned)__builtin_amdgcn_s_memrealtime();
        st_sc1(rs, 0, TOK_OFF + (hg * 64 + chunk) * 128, v4u{now, tag, now, tag});
      }
      __builtin_amdgcn_s_sleep(4);
#pragma unroll
      for (int T = 0; T < 8; ++T)
#pragma unroll
        for (int ks = 0; ks < 4; ++ks)
          vf[T][ks][1] = __builtin_bit_cast(v2u, __builtin_amdgcn_ds_read_tr16_b64_v4i16((LAS s16x4*)(rb + 65536 + ks * 16384 + (((2 * T + (pp >> 1)) ^ f) << 4))));
      bar();
      bar();
      v4u pb[4];
#pragma unroll
      for (int ks = 0; ks < 4; ++ks) pb[ks] = *(LAS v4u*)(L + K_PB + ks * 1024 + lane * 16);
      SCHED();
      const int so = PART_OFF + (16 * hg * 64 + chunk) * 1024 + w4 * 256;
#pragma unroll
      for (int hf = 0; hf < 4; ++hf) {
        f32x4 a0 = {0.f, 0.f, 0.f, 0.f}, a1 = {0.f, 0.f, 0.f, 0.f};
#pragma unroll
        for (int ks = 0; ks < 4; ++ks) {
          a0 = mfma(v4u{vf[2 * hf][ks][0].x, vf[2 * hf][ks][0].y, vf[2 * hf][ks][1].x, vf[2 * hf][ks][1].y}, pb[ks], a0);
          a1 = mfma(v4u{vf[2 * hf + 1][ks][0].x, vf[2 * hf + 1][ks][0].y, vf[2 * hf + 1][ks][1].x, vf[2 * hf + 1][ks][1].y}, pb[ks], a1);
        }
        st_sc1(rs, j * 65536 + g * 16 + hf * 64, so,
               v4u{nosent(pk_bf16(a0.x, a0.y)), nosent(pk_bf16(a0.z, a0.w)), nosent(pk_bf16(a1.x, a1.y)), nosent(pk_bf16(a1.z, a1.w))});
        SCHED();
      }
    }
    bar();
  } else {
    bar();
    while (since(tstart) < ODELAY) __builtin_amdgcn_s_sleep(2);
    {
      const v4i dv = mkdesc(p.wuv, 512 * 8192);
      const int vo = (lane >> 3) * 8192 + (((lane & 7) ^ sg) << 4);
      {
        const int rs3 = (3 - (int)((unsigned)(size_t)p.wuv >> 7)) & 7;
        dma_keep(dv, LB + O_SCR + w * 1024, (lane >> 3) * 8192 + (lane & 7) * 16, (64 * r3 + 8 * w) * 8192 + xc * 1024 + rs3 * 128);
      }
      dma_slab<TS>(mkdesc(p.wo, 4096 * 1152), (h * 128 + 64 * hb) * 1152, LB + O_WO, w, lane, 0);
#pragma unroll 1
      for (int i = 0; i < 8; ++i) {
        const int n = w + 8 * i;
        dma(dv, LB + O_WUV + n * 1024, vo, (8 * n) * 8192 + h * 256 + hb * 128);
        throttle<TS>();
      }
      drain();
    }
    bar();
    const int b = w >> 2;
    v4u uv[16], wof[8][2], wo9[1][2];
    v4u tk = {0u, tag, 0u, tag};
    if (w == 0) tk = ld_sc1(rs, lane * 128, TOK_OFF + (h >> 4) * 8192);
    SCHED();
    if (w < 4) {
      const int q = (lane >> 2) & 3, pp = lane & 3;
      const int s = ((q >> 1) & 1) | ((g & 1) << 1);
      LAS unsigned char* b0 = L + O_WUV + (8 * g + q) * 128 + 32 * (w ^ s) + 8 * pp;
#pragma unroll
      for (int ks = 0; ks < 16; ++ks) uv[ks] = tr_frag(b0 + ks * 4096, b0 + ks * 4096 + 512);
      slab_frags<8, 1>(L + O_WO, w4, lane, wo9);
    } else {
      slab_frags<0, 8>(L + O_WO, w4, lane, wof);
    }
    asm volatile("s_waitcnt lgkmcnt(0)" ::: "memory");
    {
      unsigned spin = 0;
      if (w == 0) {
#pragma unroll 1
        while (anybad(tagdiff(tk, tag) != 0) && ++spin < SPIN_CAP) {
          __builtin_amdgcn_s_sleep(PSLEEP);
          cfence();
          tk = ld_sc1(rs, lane * 128, TOK_OFF + (h >> 4) * 8192);
        }
        float tm = (float)(int)(tk.x - tstart);
        tm = mx3(tm, dpp<0x121>(tm), dpp<0x122>(tm));
        tm = mx3(tm, dpp<0x122>(tm), dpp<0x124>(tm));
        tm = mx3(tm, dpp<0x124>(tm), dpp<0x128>(tm));
        tm = mx3(tm, dpp<0x128>(tm), tm);
        tm = max_g(tm, tm);
        if (lane == 0) *(LAS unsigned*)(L + O_ST + 272) = tstart + (unsigned)(int)tm;
      }
      const int pv = g * 8192 + j * 16, ps = PART_OFF + (h * 64 + b * 32) * 1024 + w4 * 256;
      LAS u16* cd = (LAS u16*)(L + O_CC) + b * 544 + 128 * w4 + 32 * g + 16 * ((j >> 2) & 1) + (j & 3) + 8 * (j >> 3);
      bar();
      const unsigned tgo = __builtin_amdgcn_readfirstlane(*(LAS unsigned*)(L + O_ST + 272));
      v4u st[1] = {v4u{0u, tag, 0u, tag}};
#define LD_ST(i) st[0] = ld_sc1(rs, lane * 512, ST_OFF + h * 16)
      spin = 0;
      if (w == 0) {
        while (since(tgo) < S1 && ++spin < 4096u) {}
        LD_ST(0);
        cfence();
      }
      if (w == 0) {
        RETRY_TAGGED(1, st, LD_ST)
        const float ms = asf(st[0].x), ls = asf(st[0].z);
        float mx = mx3(ms, dpp<0x121>(ms), dpp<0x122>(ms));
        mx = mx3(mx, dpp<0x123>(ms), ms);
        mx = mx3(mx, dpp<0x124>(mx), ms);
        mx = mx3(mx, dpp<0x128>(mx), ms);
        float ma, mb;
        x16(mx, ma, mb);
        const float af = __builtin_amdgcn_exp2f(ms - mx3(ma, mb, ms));
        const unsigned ad = pk_bf16(af, af);
        *(LAS unsigned*)(L + O_ST + lane * 4) = ad;
        x16(row_sum16(bf_lo(ad) * ls), ma, mb);
        if ((lane & 31) == 0) *(LAS float*)(L + O_ST + 256 + (lane >> 5) * 4) = __builtin_amdgcn_rcpf(ma + mb);
      }
      spin = 0;
      while (since(tgo) < S2 && ++spin < 4096u) {}
      v4u v[8];
#pragma unroll
      for (int t = 0; t < 8; ++t) v[t] = ld_sc1(rs, pv + (t & 3) * 1024, ps + (t >> 2) * 4096);
      SCHED();
      bar();
      const v4u a0 = *(LAS v4u*)(L + O_ST + b * 128 + g * 32), a1 = *(LAS v4u*)(L + O_ST + b * 128 + g * 32 + 16);
      const float inv = *(LAS float*)(L + O_ST + 256 + b * 4);
      const unsigned ad[8] = {a0.x, a0.y, a0.z, a0.w, a1.x, a1.y, a1.z, a1.w};
      v4u bw[8];
#pragma unroll
      for (int t = 0; t < 8; ++t) bw[t] = v4u{ad[t] & dm[0], ad[t] & dm[1], ad[t] & dm[2], ad[t] & dm[3]};
      SCHED();
      f32x4 acc;
      spin = 0;
#pragma unroll 1
      for (;;) {
        unsigned mxu = 0;
        acc = f32x4{0.f, 0.f, 0.f, 0.f};
#pragma unroll
        for (int t = 0; t < 8; ++t) {
          acc = mfma(v[t], bw[t], acc);
          mxu = vmax4(v[t], mxu);
        }
        if (!anybad(mxu == SENT) || ++spin >= SPIN_CAP) break;
        unsigned bad = 0;
#pragma unroll
        for (int t = 0; t < 8; ++t) bad |= anybad(vmax4(v[t], 0u) == SENT) ? (1u << t) : 0u;
        cfence();
#pragma unroll
        for (int t = 0; t < 8; ++t)
          if (bad & (1u << t)) v[t] = ld_sc1(rs, pv + (t & 3) * 1024, ps + (t >> 2) * 4096);
      }
      const unsigned pk = pk_bf16((j < 8 ? acc.x : acc.z) * inv, (j < 8 ? acc.y : acc.w) * inv);
      cd[0] = (u16)pk;
      cd[4] = (u16)(pk >> 16);
    }
    bar();
    if (tid == 256) {
      const unsigned now = (unsigned)__builtin_amdgcn_s_memrealtime();
      st_sc1(rs, 0, OF_OFF + oi * 128, v4u{now, tag, now, tag});
    }
    if (w < 4) {
      v4u cb[16];
#pragma unroll
      for (int ks = 0; ks < 6; ++ks) cb[ks] = *(LAS v4u*)(L + O_CC + (j & 1) * 1088 + ks * 64 + g * 16);
      SCHED();
      f32x4 acc = {0.f, 0.f, 0.f, 0.f};
#pragma unroll
      for (int ks = 0; ks < 16; ++ks) {
        acc = mfma(uv[ks], cb[ks], acc);
        if (ks + 6 < 16) cb[ks + 6] = *(LAS v4u*)(L + O_CC + (j & 1) * 1088 + (ks + 6) * 64 + g * 16);
        SCHED();
      }
      if (j < 2) *(LAS v2u*)(L + O_CTX + j * 192 + (16 * w + 4 * g) * 2) = v2u{pk_bf16(acc.x, acc.y), pk_bf16(acc.z, acc.w)};
      slab_gemv<8, 1>(wo9, L + O_CTX, rs, OP_OFF + oi * 9216, w4, lane, tag);
    } else {
      slab_gemv<0, 8>(wof, L + O_CTX, rs, OP_OFF + oi * 9216, w4, lane, tag);
    }
    if (oi < 36) {
      if (w == 1) {
        v4u of;
        unsigned spin = 0;
#pragma unroll 1
        do {
          cfence();
          of = ld_sc1(rs, lane * 128, OF_OFF);
        } while (anybad(tagdiff(of, tag) != 0) && ++spin < SPIN_CAP);
        spin = 0;
#pragma unroll 1
        while (anybad(since(of.x) < D3) && ++spin < 4096u) __builtin_amdgcn_s_sleep(1);
      }
      bar();
      const float hv = reduce_lines<64>(rs, OP_OFF, oi, L + O_RED, tid, w, lane, tag) + xres;
      if (w == 0) {
        const unsigned pk = pk_bf16(hv, dpp<0xB1>(hv));
        const unsigned pk2 = (unsigned)__builtin_amdgcn_update_dpp(0, (int)pk, 0xAA, 0xf, 0xf, false);
        const float hr = bf_lo(pk);
        float sa, sb;
        x16(row_sum16(hr), sa, sb);
        const float lm = (sa + sb) * (1.0f / 32.0f), dv_ = hr - lm;
        x16(row_sum16(dv_ * dv_), sa, sb);
        if (lane <= 32 && !(lane & 3))
          st_sc1(rs, lane * 4, HB_OFF + oi * 256, v4u{lane < 32 ? pk : fbits(lm), tag, lane < 32 ? pk2 : fbits(sa + sb), tag});
      }
      if (w == 1) {
        v4u hs;
        unsigned spin = 0;
#pragma unroll 1
        do {
          __builtin_amdgcn_s_sleep(PSLEEP);
          cfence();
          hs = ld_sc1(rs, (lane < 36 ? lane : 0) * 256, HS_OFF);
        } while (anybad(tagdiff(hs, tag) != 0) && ++spin < SPIN_CAP);
      }
      bar();
      for (int i = 0; i < D5; ++i) __builtin_amdgcn_s_sleep(4);
      const float o = reduce_lines<72>(rs, FP_OFF, oi, L + O_RED, tid, w, lane, tag) + (hv + xmean);
      if (w == 0) {
        const float other = dpp<0xB1>(o);
        if (lane < 32 && !(lane & 1)) ((GAS unsigned*)p.out)[16 * oi + (lane >> 1)] = pk_bf16(o, other);
        if (oi == 0 && lane == 0) __hip_atomic_store((GAS unsigned*)p.ws, tag, __ATOMIC_RELAXED, __HIP_MEMORY_SCOPE_AGENT);
      }
    }
  }

  if (isF) {
    const int vgu = (lane >> 3) * 9216 + (((lane & 7) ^ sg) << 4);
    while (since(tstart) < FDELAY) __builtin_amdgcn_s_sleep(2);
    dma_slab<TS>(dd, 64 * fj * 1152, LB + F_SLAB, w, lane, 0);
    if (roleQ) {
      const int f9 = 9 * xc + i9, vs = (lane >> 3) * 9216 + (lane & 7) * 16;
#pragma unroll 1
      for (int n = r5 * 8 + w; n < (i9 == 0 ? 312 : 216); n += 64) {
        if (n < 72) dma_keep(dd, LB + F_G2 + w * 1024, lane * 16, 64 * f9 * 1152 + n * 1024);
        else dma_keep((n & 1) ? du : dg, LB + F_G2 + w * 1024, vs, n < 216 ? ((n - 72) >> 1) * 8 * 9216 + f9 * 128 : ((n - 168) >> 1) * 8 * 9216 + (f9 + 8) * 128);
        throttle<TS>();
      }
    }
#pragma unroll 1
    for (int i = 0; i < 3; ++i) {
      const int n = w + 8 * i;
      dma(dg, LB + F_G1 + n * 1024, vgu, n * 8 * 9216 + fj * 128);
      throttle<TS>();
      dma(du, LB + F_U1 + n * 1024, vgu, n * 8 * 9216 + fj * 128);
      throttle<TS>();
    }
    if (tid < 72) *(LAS v4u*)(L + F_HB + 2 * F_HBS + tid * 16) = v4u{0x3F803F80u, 0x3F803F80u, 0x3F803F80u, 0x3F803F80u};
    asm volatile("s_waitcnt vmcnt(6)" ::: "memory");
    bar();
#pragma unroll
    for (int i = 0; i < 2; ++i) {
      const int n = w + 8 * i;
      dma(dg, LB + F_G2 + n * 1024, vgu, (n + 24) * 8 * 9216 + fj * 128);
      dma(du, LB + F_U2 + n * 1024, vgu, (n + 24) * 8 * 9216 + fj * 128);
    }
    const int l36 = lane < 36 ? lane : 35;
    const int ho = ((36 * w + l36) >> 3) * 256 + ((36 * w + l36) & 7) * 16;
#define GATHER_H() { \
    v4u hv; \
    unsigned spin = 0; \
    _Pragma("unroll 1") \
    do { \
      __builtin_amdgcn_s_sleep(PSLEEP); \
      cfence(); \
      hv = ld_sc1(rs, ho, HB_OFF); \
    } while (anybad(tagdiff(hv, tag) != 0) && ++spin < SPIN_CAP); \
    *(LAS v2u*)(L + F_HB + (w >> 2) * F_HBS + w4 * 288 + l36 * 8) = v2u{hv.x, hv.z}; }
#define GU3_DMA() { \
    _Pragma("unroll") \
    for (int i = 0; i < 4; ++i) { \
      const int n = w + 8 * i; \
      dma(dg, LB + F_G3 + n * 1024, vgu, (n + 40) * 8 * 9216 + fj * 128); \
      dma(du, LB + F_U3 + n * 1024, vgu, (n + 40) * 8 * 9216 + fj * 128); \
      throttle<TS>(); \
    } }
    if (w < 4) {
      v4u gf[18], uf[18], wd9[1][2];
      slab_frags<8, 1>(L + F_SLAB, w4, lane, wd9);
      const int q = (lane >> 2) & 3, pp = lane & 3;
      const int s = ((q >> 1) & 1) | ((g & 1) << 1);
      LAS unsigned char* ro = L + (8 * g + q) * 128 + 32 * (w ^ s) + 8 * pp;
      bar();
      GU3_DMA()
      throttle<8>();
      bar();
#pragma unroll
      for (int ks = 0; ks < 6; ++ks) {
        gf[ks] = tr_frag(ro + F_G1 + ks * 4096, ro + F_G1 + ks * 4096 + 512);
        uf[ks] = tr_frag(ro + F_U1 + ks * 4096, ro + F_U1 + ks * 4096 + 512);
      }
#pragma unroll
      for (int ks = 0; ks < 4; ++ks) {
        gf[6 + ks] = tr_frag(ro + F_G2 + ks * 4096, ro + F_G2 + ks * 4096 + 512);
        uf[6 + ks] = tr_frag(ro + F_U2 + ks * 4096, ro + F_U2 + ks * 4096 + 512);
      }
      drain();
      bar();
#pragma unroll
      for (int ks = 0; ks < 8; ++ks) {
        gf[10 + ks] = tr_frag(ro + F_G3 + ks * 4096, ro + F_G3 + ks * 4096 + 512);
        uf[10 + ks] = tr_frag(ro + F_U3 + ks * 4096, ro + F_U3 + ks * 4096 + 512);
      }
      GATHER_H()
      bar();
      LAS unsigned char* hp = L + F_HB + ((j & 2) ? 2 : (j & 1)) * F_HBS + g * 16;
      f32x4 ag = {0.f, 0.f, 0.f, 0.f}, au = {0.f, 0.f, 0.f, 0.f};
      f32x2 ms = {0.f, 0.f};
      {
        v4u hb_[18];
#pragma unroll
        for (int ks = 0; ks < PRE; ++ks) hb_[ks] = *(LAS v4u*)(hp + ks * 64);
        SCHED();
#pragma unroll
        for (int ks = 0; ks < 18; ++ks) {
          ag = mfma(gf[ks], hb_[ks], ag);
          if (ks + PRE < 18) hb_[ks + PRE] = *(LAS v4u*)(hp + (ks + PRE) * 64);
          au = mfma(uf[ks], hb_[ks], au);
          SCHED();
          if (ks == 13) { SCHED(); bar(); ms = *(LAS f32x2*)(L + F_ST + (j & 1) * 8); SCHED(); }
        }
      }
      const int k4 = j >> 2;
      const float gsel = k4 == 0 ? ag.x : k4 == 1 ? ag.y : k4 == 2 ? ag.z : ag.w;
      const float usel = k4 == 0 ? au.x : k4 == 1 ? au.y : k4 == 2 ? au.z : au.w;
      const float gg = (gsel - ms.x * dpp<0xAA>(gsel)) * ms.y, uu = (usel - ms.x * dpp<0xAA>(usel)) * ms.y;
      const float aa = gg * __builtin_amdgcn_rcpf(1.0f + __builtin_amdgcn_exp2f(-1.4426950408889634f * gg)) * uu;
      if ((j & 2) == 0) *(LAS u16*)(L + F_ACT + (j & 1) * 192 + (16 * w + 4 * g + k4) * 2) = (u16)pk_bf16(aa, aa);
      slab_gemv<8, 1>(wd9, L + F_ACT, rs, FP_OFF + fj * 9216, w4, lane, tag);
    } else {
      v4u wdf[8][2];
      slab_frags<0, 8>(L + F_SLAB, w4, lane, wdf);
      bar();
      GU3_DMA()
      throttle<8>();
      bar();
      drain();
      bar();
      const bool in = (lane & 31) < 18;
      v4u hs = {0u, tag, 0u, tag};
      {
        const int ri = in ? 18 * (lane >> 5) + (lane & 31) : 0;
        v4u hv;
        unsigned spin = 0;
#pragma unroll 1
        do {
          __builtin_amdgcn_s_sleep(PSLEEP);
          cfence();
          hv = ld_sc1(rs, ho, HB_OFF);
          if (w == 4) hs = ld_sc1(rs, ri * 256, HS_OFF);
        } while (anybad((tagdiff(hv, tag) | tagdiff(hs, tag)) != 0) && ++spin < SPIN_CAP);
        *(LAS v2u*)(L + F_HB + F_HBS + w4 * 288 + l36 * 8) = v2u{hv.x, hv.z};
      }
      bar();
      if (w == 4) {
        float sa, sb;
        x16(row_sum16(in ? asf(hs.x) : 0.f), sa, sb);
        const float mean = (sa + sb) * (1.0f / 18.0f), dv_ = asf(hs.x) - mean;
        x16(row_sum16(in ? asf(hs.z) + 32.0f * dv_ * dv_ : 0.f), sa, sb);
        if ((lane & 31) == 0) *(LAS f32x2*)(L + F_ST + (lane >> 5) * 8) = f32x2{mean, __builtin_amdgcn_rsqf((sa + sb) * (1.0f / M) + 1e-5f)};
      }
      bar();
      slab_gemv<0, 8>(wdf, L + F_ACT, rs, FP_OFF + fj * 9216, w4, lane, tag);
    }
  }
}
}

extern "C" void kernel_launch(void* const* d_in, const int* in_sizes, int n_in, void* d_out, int out_size, void* d_ws,
                              size_t ws_size, hipStream_t stream) {
  Params p;
  p.x = (const u16*)d_in[0];
  p.ckv = (const u16*)d_in[1];
  p.wq = (const u16*)d_in[2];
  p.wuk = (const u16*)d_in[3];
  p.wuv = (const u16*)d_in[4];
  p.wo = (const u16*)d_in[5];
  p.wg = (const u16*)d_in[7];
  p.wu = (const u16*)d_in[8];
  p.wd = (const u16*)d_in[9];
  p.out = (u16*)d_out;
  p.ws = (unsigned char*)d_ws;
  (void)hipFuncSetAttribute((const void*)fused_kernel, hipFuncAttributeMaxDynamicSharedMemorySize, LDS_BYTES);
  hipLaunchKernelGGL(fused_kernel, dim3(256), dim3(NTHR), LDS_BYTES, stream, p);
}
```
